```python
import torch

def _softmax(x, axis=-1):
    return torch.softmax(x, dim=axis)
def _gelu(x):
    return torch.nn.functional.gelu(x, approximate="tanh")
def _silu(x):
    return torch.nn.functional.silu(x)
def _sigmoid(x):
    return torch.sigmoid(x)
def _layernorm(x, axis=-1, eps=1e-5):
    m = x.mean(dim=axis, keepdim=True)
    v = x.var(dim=axis, keepdim=True, unbiased=False)
    return (x - m) / torch.sqrt(v + eps)
def _scatter_add(tgt, idx, src, axis=0):
    return tgt.clone().index_add_(axis, idx, src)
def _segment_sum(src, seg, n):
    out = torch.zeros((n,) + src.shape[1:], dtype=src.dtype)
    return out.index_add_(0, seg, src)
def _pad(x, pw):
    flat = tuple(v for pair in reversed(pw) for v in pair)
    return torch.nn.functional.pad(x, flat)

kernel_name = 'recurrent_scan__sliding_block_cached__geglu_ffn__decode'

def setup_inputs():

    _w = (lambda a: (lambda t: t.to(torch.bfloat16) if t.is_floating_point() else t)(torch.as_tensor(a)))
    def _A_setup():
        B, Sq, D = 1, 1, 640
        Hq, Hkv, Sk, Dh = 32, 32, 32768, 192
        assert Hq % Hkv == 0, (Hq, Hkv)
        return {
            "x": _w((lambda *s: torch.randn(*s, dtype=torch.bfloat16) * 0.05)(B, Sq, D)),
            "k_cache": _w((lambda *s: torch.randn(*s, dtype=torch.bfloat16) * 1.6)(B, Hkv, Sk, Dh)),
            "v_cache": _w((lambda *s: torch.randn(*s, dtype=torch.bfloat16) * 0.2)(B, Hkv, Sk, Dh)),
            "wq": _w((lambda *s: torch.randn(*s, dtype=torch.bfloat16) * 1.6)(D, Hq * Dh) * (D ** -0.5)),
            "wo": _w((lambda *s: torch.randn(*s, dtype=torch.bfloat16) * 0.2)(Hq * Dh, D) * ((Hq * Dh) ** -0.5)),
        }
    _A_in = _A_setup()

    def _B_setup():
        B, S, D, F = 1, 1, 640, 1536
        return {
            "x": _w((lambda *s: torch.randn(*s, dtype=torch.bfloat16) * 0.05)(B, S, D)),
            "w_gate": _w((lambda *s: torch.randn(*s, dtype=torch.bfloat16) * 0.1)(D, F)),
            "w_up": _w((lambda *s: torch.randn(*s, dtype=torch.bfloat16) * 0.1)(D, F)),
            "w_down": _w((lambda *s: torch.randn(*s, dtype=torch.bfloat16) * 0.1)(F, D)),
        }
    _B_in = _B_setup()

    return {
        "A_x": _A_in["x"],
        "A_k_cache": _A_in["k_cache"],
        "A_v_cache": _A_in["v_cache"],
        "A_wq": _A_in["wq"],
        "A_wo": _A_in["wo"],
        "B_x": _B_in["x"],
        "B_w_gate": _B_in["w_gate"],
        "B_w_up": _B_in["w_up"],
        "B_w_down": _B_in["w_down"],
    }

def reference(A_x, A_k_cache, A_v_cache, A_wq, A_wo, B_x, B_w_gate, B_w_up, B_w_down):
    def _A_ref(x, k_cache, v_cache, wq, wo):
        B, Sq, D = x.shape
        Hq, Dh = 32, 192
        Hkv, Sk = k_cache.shape[1], k_cache.shape[2]
        W = min(4096, max(256, Sk // 4))
        rep = Hq // Hkv
        q = (lambda x, *d: x.permute(*d))((x @ wq).reshape(B, Sq, Hq, Dh), 0, 2, 1, 3)
        k = (lambda x, n, axis: torch.repeat_interleave(x, n, dim=axis))(k_cache[:, :, Sk - W:, :], rep, axis=1)
        v = (lambda x, n, axis: torch.repeat_interleave(x, n, dim=axis))(v_cache[:, :, Sk - W:, :], rep, axis=1)
        scores = torch.einsum("bhqd,bhkd->bhqk", q, k) * (Dh ** -0.5)
        p = _softmax(scores, axis=-1)
        o = torch.einsum("bhqk,bhkd->bhqd", p, v)
        return (lambda x, *d: x.permute(*d))(o, 0, 2, 1, 3).reshape(B, Sq, Hq * Dh) @ wo

    def _B_ref(x, w_gate, w_up, w_down):
        return (_gelu(x @ w_gate) * (x @ w_up)) @ w_down

    x = A_x
    xn = _layernorm(x, axis=-1)
    s = _A_ref(xn, A_k_cache, A_v_cache, A_wq, A_wo)
    g = _silu(_B_ref(xn, B_w_gate, B_w_up, B_w_down))
    return x + s * g
```

```cpp
#include <hip/hip_runtime.h>
#include <hip/hip_bf16.h>
#include <stdint.h>

#define EMU 0

typedef unsigned short u16;
typedef unsigned v4u __attribute__((ext_vector_type(4)));

constexpr int D = 640, H = 32, DH = 192, SK = 32768, W = 4096, F = 1536, HD = H * DH;
constexpr int NS = 8, KPS = W / NS;

__device__ __forceinline__ float bf2f(u16 b) { return __uint_as_float((unsigned)b << 16); }
__device__ __forceinline__ float rbf(float f) { return __bfloat162float(__float2bfloat16(f)); }
#define RB(x) (x)
__device__ __forceinline__ float lo(unsigned u) { return __uint_as_float(u << 16); }
__device__ __forceinline__ float hi(unsigned u) { return __uint_as_float(u & 0xffff0000u); }

__device__ void layernorm_to_lds(const u16* x, float* xn, float* red) {
  int tid = threadIdx.x;
  float s = 0.f;
  for (int i = tid; i < D; i += blockDim.x) s += bf2f(x[i]);
  for (int o = 32; o; o >>= 1) s += __shfl_xor(s, o);
  if ((tid & 63) == 0) red[tid >> 6] = s;
  __syncthreads();
  float tot = 0.f;
  for (int w = 0; w < (int)blockDim.x / 64; ++w) tot += red[w];
  float mean = tot / D;
  __syncthreads();
  float v = 0.f;
  for (int i = tid; i < D; i += blockDim.x) { float d = bf2f(x[i]) - mean; v += d * d; }
  for (int o = 32; o; o >>= 1) v += __shfl_xor(v, o);
  if ((tid & 63) == 0) red[tid >> 6] = v;
  __syncthreads();
  float vt = 0.f;
  for (int w = 0; w < (int)blockDim.x / 64; ++w) vt += red[w];
  float var = RB(vt / D);
  float mb = RB(mean);
  float sd = RB(sqrtf(RB(var + 1e-5f)));
  for (int i = tid; i < D; i += blockDim.x) xn[i] = RB(RB(bf2f(x[i]) - mb) / sd);
  __syncthreads();
}

__global__ void __launch_bounds__(256) k_proj(const u16* x, const u16* wq, const u16* wg, const u16* wu, float* q, float* gate, float* up) {
  __shared__ float xn[D];
  __shared__ float red[4];
  __shared__ float part[32][64];
  layernorm_to_lds(x, xn, red);
  int b = blockIdx.x;
  const u16* w; int ld; float* out; int c0;
  if (b < 96) { w = wq; ld = HD; out = q; c0 = b * 64; }
  else if (b < 120) { w = wg; ld = F; out = gate; c0 = (b - 96) * 64; }
  else { w = wu; ld = F; out = up; c0 = (b - 120) * 64; }
  int r = threadIdx.x >> 3, c8 = threadIdx.x & 7;
  float acc[8] = {0};
  for (int i = r; i < D; i += 32) {
    v4u v = *(const v4u*)(w + (size_t)i * ld + c0 + c8 * 8);
    float xv = xn[i];
    acc[0] += xv * lo(v.x); acc[1] += xv * hi(v.x);
    acc[2] += xv * lo(v.y); acc[3] += xv * hi(v.y);
    acc[4] += xv * lo(v.z); acc[5] += xv * hi(v.z);
    acc[6] += xv * lo(v.w); acc[7] += xv * hi(v.w);
  }
  for (int j = 0; j < 8; ++j) part[r][c8 * 8 + j] = acc[j];
  __syncthreads();
  if (threadIdx.x < 64) {
    float s = 0.f;
    for (int i = 0; i < 32; ++i) s += part[i][threadIdx.x];
    out[c0 + threadIdx.x] = RB(s);
  }
}

__global__ void __launch_bounds__(256) k_scores(const u16* kc, const float* q, float* sc) {
  int h = blockIdx.x >> 3, s = blockIdx.x & 7;
  __shared__ float qs[DH];
  if (threadIdx.x < DH) qs[threadIdx.x] = q[h * DH + threadIdx.x];
  __syncthreads();
  int g = threadIdx.x >> 3, c8 = threadIdx.x & 7;
  const u16* kb = kc + ((size_t)h * SK + (SK - W) + s * KPS) * DH;
  for (int k = g; k < KPS; k += 32) {
    float a = 0.f;
    for (int t = 0; t < 3; ++t) {
      v4u v = *(const v4u*)(kb + (size_t)k * DH + t * 64 + c8 * 8);
      const float* qq = qs + t * 64 + c8 * 8;
      a += qq[0] * lo(v.x) + qq[1] * hi(v.x) + qq[2] * lo(v.y) + qq[3] * hi(v.y) + qq[4] * lo(v.z) + qq[5] * hi(v.z) + qq[6] * lo(v.w) + qq[7] * hi(v.w);
    }
    a += __shfl_xor(a, 1); a += __shfl_xor(a, 2); a += __shfl_xor(a, 4);
    if (c8 == 0) sc[h * W + s * KPS + k] = RB(RB(a) * 0.07216878364870322f);
  }
}

__global__ void __launch_bounds__(256) k_pv(const u16* vc, const float* sc, float* opart) {
  int h = blockIdx.x >> 3, s = blockIdx.x & 7;
  __shared__ float red[4];
  __shared__ float p[KPS];
  __shared__ float part[32][DH];
  int tid = threadIdx.x;
  float m = -INFINITY;
  for (int i = tid; i < W; i += 256) m = fmaxf(m, sc[h * W + i]);
  for (int o = 32; o; o >>= 1) m = fmaxf(m, __shfl_xor(m, o));
  if ((tid & 63) == 0) red[tid >> 6] = m;
  __syncthreads();
  m = fmaxf(fmaxf(red[0], red[1]), fmaxf(red[2], red[3]));
  __syncthreads();
  float l = 0.f;
  for (int i = tid; i < W; i += 256) l += __expf(sc[h * W + i] - m);
  for (int o = 32; o; o >>= 1) l += __shfl_xor(l, o);
  if ((tid & 63) == 0) red[tid >> 6] = l;
  __syncthreads();
  l = red[0] + red[1] + red[2] + red[3];
  for (int i = tid; i < KPS; i += 256) p[i] = RB(__expf(sc[h * W + s * KPS + i] - m) / l);
  __syncthreads();
  int g = tid >> 3, c8 = tid & 7;
  const u16* vb = vc + ((size_t)h * SK + (SK - W) + s * KPS) * DH;
  float acc[24] = {0};
  for (int k = g; k < KPS; k += 32) {
    float pk = p[k];
    for (int t = 0; t < 3; ++t) {
      v4u v = *(const v4u*)(vb + (size_t)k * DH + t * 64 + c8 * 8);
      acc[t * 8 + 0] += pk * lo(v.x); acc[t * 8 + 1] += pk * hi(v.x);
      acc[t * 8 + 2] += pk * lo(v.y); acc[t * 8 + 3] += pk * hi(v.y);
      acc[t * 8 + 4] += pk * lo(v.z); acc[t * 8 + 5] += pk * hi(v.z);
      acc[t * 8 + 6] += pk * lo(v.w); acc[t * 8 + 7] += pk * hi(v.w);
    }
  }
  for (int t = 0; t < 3; ++t) for (int j = 0; j < 8; ++j) part[g][t * 64 + c8 * 8 + j] = acc[t * 8 + j];
  __syncthreads();
  if (tid < DH) {
    float a = 0.f;
    for (int i = 0; i < 32; ++i) a += part[i][tid];
    opart[(h * NS + s) * DH + tid] = a;
  }
}

__device__ __forceinline__ float gelu_tanh(float x) {
  float u = 0.7978845608028654f * (x + 0.044715f * x * x * x);
  return 0.5f * x * (1.f + tanhf(u));
}

__global__ void __launch_bounds__(256) k_out1(const u16* wo, const u16* wd, const float* opart, const float* gate, const float* up, float* ypart, float* zpart) {
  int b = blockIdx.x;
  __shared__ float o[24];
  __shared__ float hh[6];
  int tid = threadIdx.x;
  if (tid < 24) {
    int row = b * 24 + tid; int h = row / DH, d = row % DH;
    float a = 0.f;
    for (int s = 0; s < NS; ++s) a += opart[(h * NS + s) * DH + d];
    o[tid] = RB(a);
  }
  if (tid >= 64 && tid < 70) {
    int j = b * 6 + tid - 64;
    hh[tid - 64] = RB(RB(gelu_tanh(gate[j])) * up[j]);
  }
  __syncthreads();
  for (int c = tid; c < D; c += 256) {
    float y = 0.f, z = 0.f;
    for (int r = 0; r < 24; ++r) y += o[r] * bf2f(wo[(size_t)(b * 24 + r) * D + c]);
    for (int r = 0; r < 6; ++r) z += hh[r] * bf2f(wd[(size_t)(b * 6 + r) * D + c]);
    ypart[b * D + c] = y; zpart[b * D + c] = z;
  }
}

__global__ void __launch_bounds__(256) k_out2(const u16* x, const float* ypart, const float* zpart, __hip_bfloat16* out) {
  int c = blockIdx.x * 4 + (threadIdx.x >> 6), lane = threadIdx.x & 63;
  float y = 0.f, z = 0.f;
  for (int i = lane; i < 256; i += 64) { y += ypart[i * D + c]; z += zpart[i * D + c]; }
  for (int o = 32; o; o >>= 1) { y += __shfl_xor(y, o); z += __shfl_xor(z, o); }
  if (lane == 0) {
    float s = RB(y), zz = RB(z);
    float g = RB(zz / (1.f + __expf(-zz)));
    out[c] = __float2bfloat16(bf2f(x[c]) + RB(s * g));
  }
}

extern "C" void kernel_launch(void* const* d_in, const int* in_sizes, int n_in, void* d_out, int out_size, void* d_ws, size_t ws_size, hipStream_t stream) {
  const u16* x = (const u16*)d_in[0];
  const u16* kc = (const u16*)d_in[1];
  const u16* vc = (const u16*)d_in[2];
  const u16* wq = (const u16*)d_in[3];
  const u16* wo = (const u16*)d_in[4];
  const u16* wg = (const u16*)d_in[6];
  const u16* wu = (const u16*)d_in[7];
  const u16* wd = (const u16*)d_in[8];
  float* ws = (float*)d_ws;
  float* q = ws; float* gate = q + HD; float* up = gate + F; float* sc = up + F;
  float* opart = sc + H * W; float* ypart = opart + 256 * DH; float* zpart = ypart + 256 * D;
  k_proj<<<144, 256, 0, stream>>>(x, wq, wg, wu, q, gate, up);
  k_scores<<<256, 256, 0, stream>>>(kc, q, sc);
  k_pv<<<256, 256, 0, stream>>>(vc, sc, opart);
  k_out1<<<256, 256, 0, stream>>>(wo, wd, opart, gate, up, ypart, zpart);
  k_out2<<<160, 256, 0, stream>>>(x, ypart, zpart, (__hip_bfloat16*)d_out);
}
```

```cpp
#include <hip/hip_runtime.h>
#include <hip/hip_bf16.h>

typedef unsigned short u16;
typedef unsigned v4u __attribute__((ext_vector_type(4)));
typedef unsigned v16u __attribute__((ext_vector_type(16)));
typedef float f32x4 __attribute__((ext_vector_type(4)));
typedef float f32x2 __attribute__((ext_vector_type(2)));
typedef __bf16 bf16x8 __attribute__((ext_vector_type(8)));
typedef __bf16 bf16x2 __attribute__((ext_vector_type(2)));
typedef __amdgpu_buffer_rsrc_t rsrc_t;
typedef unsigned long long u64;

constexpr int D = 640, NH = 32, DH = 192, SK = 32768, WIN = 4096, FF = 1536;
constexpr int AUX_NT = 2, AUX_SC1 = 16;
constexpr int OOB = 0x40000000;
constexpr unsigned SPIN_CAP = 1u << 16;

constexpr int WS_EPOCH = 0;
constexpr int P_STRIDE = 1664, Y_STRIDE = 1152;
constexpr int WS_T0 = 2048;
constexpr int WS_ML = 4096;
constexpr int WS_QP = 8192;
constexpr int WS_OP = WS_QP + 240 * P_STRIDE;
constexpr int WS_ZP = WS_OP + 256 * P_STRIDE;
constexpr int WS_YP = WS_ZP + 240 * Y_STRIDE;
constexpr int WS_END = WS_YP + 240 * Y_STRIDE;

#define TAU 1376
#define LEAD -158
#define PWAIT 1

__device__ __forceinline__ rsrc_t mk(const void* p, int bytes) { return __builtin_amdgcn_make_buffer_rsrc((void*)p, 0, bytes, 0x00020000); }
template <int AUX> __device__ __forceinline__ v4u ld16(rsrc_t r, int voff, int soff = 0) { return __builtin_amdgcn_raw_buffer_load_b128(r, voff, soff, AUX); }
__device__ __forceinline__ float asf(unsigned u) { return __builtin_bit_cast(float, u); }
__device__ __forceinline__ unsigned asu(float f) { return __builtin_bit_cast(unsigned, f); }
__device__ __forceinline__ void publish(rsrc_t r, int voff, f32x2 v, unsigned tag) {
  __builtin_amdgcn_raw_buffer_store_b128(v4u{asu(v.x), tag, asu(v.y), tag}, r, voff, 0, AUX_SC1);
}
__device__ __forceinline__ float bflo(unsigned u) { return asf(u << 16); }
__device__ __forceinline__ float bfhi(unsigned u) { return asf(u & 0xffff0000u); }
__device__ __forceinline__ unsigned pk(float lo, float hi) {
  bf16x2 r = {(__bf16)lo, (__bf16)hi};
  return __builtin_bit_cast(unsigned, r);
}
__device__ __forceinline__ float rb(float f) { return (float)(__bf16)f; }
__device__ __forceinline__ float dot2(unsigned a, unsigned b, float c) {
  return __builtin_amdgcn_fdot2_f32_bf16(__builtin_bit_cast(bf16x2, a), __builtin_bit_cast(bf16x2, b), c, false);
}
template <int CTRL> __device__ __forceinline__ float dpp(float x) {
  return asf((unsigned)__builtin_amdgcn_mov_dpp((int)asu(x), CTRL, 0xf, 0xf, true));
}
constexpr int XOR1 = 0xB1, XOR2 = 0x4E, HMIR = 0x141, ROR8 = 0x128, ROR4 = 0x124, BCAST = 0x150;
__device__ __forceinline__ float sum8(float x) { x += dpp<XOR1>(x); x += dpp<XOR2>(x); x += dpp<HMIR>(x); return x; }
__device__ __forceinline__ float max8(float x) { x = fmaxf(x, dpp<XOR1>(x)); x = fmaxf(x, dpp<XOR2>(x)); x = fmaxf(x, dpp<HMIR>(x)); return x; }
__device__ __forceinline__ float sum_rows(float x) {
  auto s = __builtin_amdgcn_permlane16_swap(asu(x), asu(x), false, false);
  x = asf(s[0]) + asf(s[1]);
  auto t = __builtin_amdgcn_permlane32_swap(asu(x), asu(x), false, false);
  return asf(t[0]) + asf(t[1]);
}
__device__ __forceinline__ float max_rows(float x) {
  auto s = __builtin_amdgcn_permlane16_swap(asu(x), asu(x), false, false);
  x = fmaxf(asf(s[0]), asf(s[1]));
  auto t = __builtin_amdgcn_permlane32_swap(asu(x), asu(x), false, false);
  return fmaxf(asf(t[0]), asf(t[1]));
}
__device__ __forceinline__ float sum_u(float x) { x += dpp<ROR8>(x); return sum_rows(x); }
__device__ __forceinline__ float max_u(float x) { x = fmaxf(x, dpp<ROR8>(x)); return max_rows(x); }
__device__ __forceinline__ float wave_sum(float x) { return sum_u(sum8(x)); }
__device__ __forceinline__ float sum_rows_xy(float x, float y) {
  auto s = __builtin_amdgcn_permlane16_swap(asu(x), asu(y), false, false);
  const float t = asf(s[0]) + asf(s[1]);
  auto r = __builtin_amdgcn_permlane32_swap(asu(t), asu(t), false, false);
  return asf(r[0]) + asf(r[1]);
}
__device__ __forceinline__ f32x2 sum_rows2(f32x2 v) {
  auto a = __builtin_amdgcn_permlane16_swap(asu(v.x), asu(v.x), false, false), b = __builtin_amdgcn_permlane16_swap(asu(v.y), asu(v.y), false, false);
  v = f32x2{asf(a[0]), asf(b[0])} + f32x2{asf(a[1]), asf(b[1])};
  auto c = __builtin_amdgcn_permlane32_swap(asu(v.x), asu(v.x), false, false), d = __builtin_amdgcn_permlane32_swap(asu(v.y), asu(v.y), false, false);
  return f32x2{asf(c[0]), asf(d[0])} + f32x2{asf(c[1]), asf(d[1])};
}
__device__ __forceinline__ v4u diag(float val, const v4u& mask) { const unsigned p = pk(val, val); return v4u{p, p, p, p} & mask; }
template <int L> __device__ __forceinline__ v4u diag_lane(unsigned p, const v4u& mask) {
  const unsigned d = (unsigned)__builtin_amdgcn_mov_dpp((int)p, BCAST + L, 0xf, 0xf, true);
  return v4u{d, d, d, d} & mask;
}
__device__ __forceinline__ f32x4 mfma(v4u a, v4u b, f32x4 c) {
  return __builtin_amdgcn_mfma_f32_16x16x32_bf16(__builtin_bit_cast(bf16x8, a), __builtin_bit_cast(bf16x8, b), c, 0, 0, 0);
}
#define PIN() __builtin_amdgcn_sched_barrier(0)
__device__ __forceinline__ void wg_barrier() {
  asm volatile("s_waitcnt lgkmcnt(0)" ::: "memory");
  __builtin_amdgcn_s_barrier();
  asm volatile("" ::: "memory");
}
__device__ __forceinline__ void lockstep() { PIN(); __builtin_amdgcn_s_barrier(); PIN(); }
template <int N> __device__ __forceinline__ void pace() { PIN(); asm volatile("s_waitcnt vmcnt(%0)" :: "i"(N)); PIN(); }
__device__ __forceinline__ unsigned clock_lo() {
  u64 t;
  asm volatile("s_memrealtime %0\n\ts_waitcnt lgkmcnt(0)" : "=s"(t) :: "memory");
  return (unsigned)t;
}
__device__ __forceinline__ void not_before(unsigned t) {
#pragma unroll 1
  while ((int)(clock_lo() - t) < 0) __builtin_amdgcn_s_sleep(1);
  PIN();
}
__device__ __forceinline__ bool tagged(const v4u& g, unsigned tag) { return (g.y == tag) & (g.w == tag); }
__device__ __forceinline__ u64 missing(const v4u& g, unsigned tag) { return __builtin_amdgcn_ballot_w64(g.y != tag) | __builtin_amdgcn_ballot_w64(g.w != tag); }
__device__ __forceinline__ void reload1(v4u& a, rsrc_t rs, int oa) {
  asm volatile("buffer_load_dwordx4 %0, %1, %2, 0 offen sc1\n\ts_waitcnt vmcnt(0)" : "=&v"(a) : "v"(oa), "s"(rs) : "memory");
}
__device__ __forceinline__ void reload2(v4u& a, v4u& b, rsrc_t rs, int oa, int ob) {
  asm volatile("buffer_load_dwordx4 %0, %2, %4, 0 offen sc1\n\tbuffer_load_dwordx4 %1, %3, %4, 0 offen sc1\n\ts_waitcnt vmcnt(0)"
               : "=&v"(a), "=&v"(b) : "v"(oa), "v"(ob), "s"(rs) : "memory");
}
__device__ __forceinline__ void reload3(v4u& a, v4u& b, v4u& c, rsrc_t rs, int oa, int ob, int oc) {
  asm volatile("buffer_load_dwordx4 %0, %3, %6, 0 offen sc1\n\tbuffer_load_dwordx4 %1, %4, %6, 0 offen sc1\n\t"
               "buffer_load_dwordx4 %2, %5, %6, 0 offen sc1\n\ts_waitcnt vmcnt(0)"
               : "=&v"(a), "=&v"(b), "=&v"(c) : "v"(oa), "v"(ob), "v"(oc), "s"(rs) : "memory");
}
__device__ __forceinline__ float gelu_tanh(float x) {
  const float u = 0.7978845608028654f * (x + 0.044715f * x * x * x);
  const float e = __builtin_amdgcn_exp2f(u * 2.885390081777927f);
  const float th = 1.f - 2.f / (e + 1.f);
  return 0.5f * x * (1.f + th);
}
__device__ __forceinline__ float colsum3(const v4u& a, const v4u& b, const v4u& c) {
  float t0 = (asf(a.x) + asf(b.x)) + asf(c.x), t1 = (asf(a.z) + asf(b.z)) + asf(c.z);
  t0 += dpp<ROR4>(t0); t1 += dpp<ROR4>(t1);
  t0 += dpp<ROR8>(t0); t1 += dpp<ROR8>(t1);
  return rb(sum_rows_xy(t0, t1));
}

constexpr int L_RED = 0;
constexpr int L_RED2 = 16 * 192;
constexpr int L_Q = L_RED2 + 16 * 128;
constexpr int L_GU = L_Q + 96;
constexpr int L_WM = L_GU + 64;
constexpr int L_WL = L_WM + 8;
constexpr int L_DT = L_WL + 8;
constexpr int L_SC = L_DT + 1;
constexpr int L_END = 21504;

__global__ void __launch_bounds__(512, 2)
fused_decode(const u16* x_, const u16* kc_, const u16* vc_, const u16* wq_, const u16* wo_, const u16* wg_, const u16* wu_,
             const u16* wd_, u16* out_, unsigned char* ws_) {
  __shared__ __attribute__((aligned(16))) float lds[L_END];
  const int b = blockIdx.x, xg = b & 7, idx = b >> 3, h = 4 * xg + (idx & 3), s = idx >> 2;
  const int tid = threadIdx.x, lane = tid & 63, w = __builtin_amdgcn_readfirstlane(tid >> 6);
  const int u = lane >> 3, c8 = lane & 7, g = lane >> 4, n16 = lane & 15;
  v16u ka; v4u kb;
  asm volatile("s_load_dwordx16 %0, %2, 0x0\n\ts_load_dwordx4 %1, %2, 0x40" : "=&s"(ka), "=&s"(kb) : "s"(__builtin_amdgcn_kernarg_segment_ptr()));
  PIN();
  const int rgl = idx / 5, cb = idx - 5 * rgl;
  const bool tile = idx < 30;
  int astride = rgl < 4 ? NH * DH * 2 : FF * 2;
  int aoff = 128 * cb * astride + (rgl < 4 ? 1536 * xg + 384 * rgl : 384 * xg);
  int abytes = tile ? 127 * astride + 384 : 0;
  int asel = rgl < 4 ? 0 : rgl - 3;
  int xro = (128 * cb + 16 * w + 2 * u) * 2;
  int ao = (16 * w + 2 * u) * astride + c8 * 16;
  unsigned kvoff = ((unsigned)h * SK + (SK - WIN)) * (DH * 2);
  int kvo = u * 384 + c8 * 16, kvs = w * 3072 + s * 24576;
  asm volatile("" : "+s"(astride), "+s"(aoff), "+s"(abytes), "+s"(asel), "+s"(kvoff), "+s"(kvs), "+v"(xro), "+v"(ao), "+v"(kvo));
  PIN();
  asm volatile("s_waitcnt lgkmcnt(0)" : "+s"(ka), "+s"(kb));
#define KARG(lo, hi) (((u64)(hi) << 32) | (lo))
  const u16 *x = (const u16*)KARG(ka[0], ka[1]), *kc = (const u16*)KARG(ka[2], ka[3]), *vc = (const u16*)KARG(ka[4], ka[5]);
  const u16 *wq = (const u16*)KARG(ka[6], ka[7]), *wo = (const u16*)KARG(ka[8], ka[9]), *wg = (const u16*)KARG(ka[10], ka[11]);
  const u16 *wu = (const u16*)KARG(ka[12], ka[13]), *wd = (const u16*)KARG(ka[14], ka[15]);
  u16* out = (u16*)KARG(kb[0], kb[1]);
  unsigned char* ws = (unsigned char*)KARG(kb[2], kb[3]);

  const rsrc_t ra = mk((const char*)(asel == 0 ? wq : (asel == 1 ? wg : wu)) + aoff, abytes);
  v4u a[6];
#pragma unroll
  for (int t = 0; t < 3; ++t) a[t] = ld16<AUX_NT>(ra, ao + t * 128);
  PIN();
  const rsrc_t rx = mk(x, D * 2);
  const v4u xs = ld16<0>(rx, lane * 16);
  const unsigned xs4 = __builtin_amdgcn_raw_buffer_load_b32(rx, lane * 4 + 1024, 0, 0);
  const unsigned xr = __builtin_amdgcn_raw_buffer_load_b32(rx, xro, 0, 0);
  PIN();
#pragma unroll
  for (int t = 0; t < 3; ++t) a[3 + t] = ld16<AUX_NT>(ra, ao + t * 128, astride);
  PIN();
  const rsrc_t rws = mk(ws, WS_END);
  const unsigned epoch = __builtin_amdgcn_raw_buffer_load_b32(rws, WS_EPOCH, 0, AUX_SC1);
  const rsrc_t rk = mk((const char*)kc + kvoff, WIN * DH * 2), rv = mk((const char*)vc + kvoff, WIN * DH * 2);
  lockstep();
  v4u kv[48];
#define ISSUE(n) do { _Pragma("unroll") for (int t_ = 0; t_ < 3; ++t_) { \
    kv[3 * (n) + t_] = ld16<AUX_NT>((n) < 8 ? rk : rv, kvo + t_ * 128, kvs + (((n) + h) & 7) * 196608); PIN(); } } while (0)
  ISSUE(0);
  v4u mask;
  {
    const unsigned hm = (lane & 1) ? 0xffff0000u : 0x0000ffffu;
    const int r = c8 >> 1;
    mask = v4u{r == 0 ? hm : 0u, r == 1 ? hm : 0u, r == 2 ? hm : 0u, r == 3 ? hm : 0u};
  }
  pace<10>();
  ISSUE(1);
  float xn0, xn1;
  {
    const unsigned xv[5] = {xs.x, xs.y, xs.z, xs.w, xs4};
    float sum = 0.f;
#pragma unroll
    for (int j = 0; j < 5; ++j) sum += bflo(xv[j]) + bfhi(xv[j]);
    const float mean = wave_sum(sum) * (1.f / D);
    float sq = 0.f;
#pragma unroll
    for (int j = 0; j < 5; ++j) { const float d0 = bflo(xv[j]) - mean, d1 = bfhi(xv[j]) - mean; sq += d0 * d0 + d1 * d1; }
    const float var = rb(wave_sum(sq) * (1.f / D));
    const float mb = rb(mean), sd = rb(sqrtf(rb(var + 1e-5f)));
    xn0 = rb(bflo(xr) - mb) / sd;
    xn1 = rb(bfhi(xr) - mb) / sd;
  }
  const bool valid = ((lane >> 3) & 1) == (lane >> 5);
  float* const redp = lds + L_RED + (w * 2 + (lane >> 5)) * 192 + 8 * c8 + 4 * (g & 1);
  {
    const v4u d0 = diag(xn0, mask), d1 = diag(xn1, mask);
    f32x4 acc[3];
#pragma unroll
    for (int t = 0; t < 3; ++t) acc[t] = mfma(d0, a[t], f32x4{0.f, 0.f, 0.f, 0.f});
#pragma unroll
    for (int t = 0; t < 3; ++t) acc[t] = mfma(d1, a[3 + t], acc[t]);
    if (valid) {
#pragma unroll
      for (int t = 0; t < 3; ++t) *(f32x4*)(redp + 64 * t) = acc[t];
    }
    PIN();
  }
  ISSUE(2);
  const unsigned tag = (unsigned)__builtin_amdgcn_readfirstlane((int)epoch) * 0x9E3779B1u + 0x7F4A7C15u;
  wg_barrier();
  const float* const sump = lds + L_RED + 4 * g * 192 + 32 * w + 2 * n16;
#define SUM192() sum_rows2((*(const f32x2*)sump + *(const f32x2*)(sump + 192)) + (*(const f32x2*)(sump + 384) + *(const f32x2*)(sump + 576)))
  unsigned tref;
  {
    f32x2 sum = SUM192();
    tref = clock_lo();
    if (w == 6) sum.x = asf(tref);
    publish(rws, !tile ? OOB : ((w < 6 && lane < 16) ? WS_QP + (xg * 30 + idx) * P_STRIDE + (16 * w + lane) * 16
                                                     : ((tid == 384 && cb == 2) ? WS_T0 + (rgl * 8 + xg) * 16 : OOB)), sum, tag);
  }
  PIN();
  const int yb = (cb * 48 + 6 * xg + rgl) * Y_STRIDE;
  const int ob = WS_OP + (h * 8 + s) * P_STRIDE;
  const rsrc_t rd = mk(wd + (size_t)(32 * (6 * xg + rgl)) * D + 128 * cb, tile ? 31 * D * 2 + 256 : 0);
  const rsrc_t ro = mk(wo + (size_t)(128 * (6 * xg + rgl)) * D + 128 * cb, tile ? 127 * D * 2 + 256 : 0);
  const bool glive = w < 6 || tile;
  const int gb = WS_QP + (xg * 30 + 5 * (w < 6 ? (idx & 3) : w - 2)) * P_STRIDE + (16 * (w < 6 ? w : rgl) + n16) * 16;
  const int go0 = glive ? gb + g * P_STRIDE : OOB;
  const int wl = 48 * w + lane - 16;
  const int go1 = g == 0 ? (glive ? gb + 4 * P_STRIDE : OOB)
                         : (wl < 96 ? ob + wl * 16
                                    : (wl < 224 ? (tile ? (wl < 160 ? WS_YP - 96 * 16 : WS_ZP - 160 * 16) + yb + wl * 16 : OOB)
                                                : (w == 7 ? WS_T0 + (lane - 16) * 16 : OOB)));
  pace<7>(); ISSUE(3); lockstep();
  pace<7>(); ISSUE(4); lockstep();
  pace<7>();
  v4u g0 = ld16<AUX_SC1>(rws, go0), g1 = ld16<AUX_SC1>(rws, go1);
  lockstep();
  ISSUE(5); lockstep();
  pace<8>(); ISSUE(6); lockstep();
  pace<6>(); ISSUE(7);
#define GATHER_OK() (!glive || (missing(g0, tag) | (missing(g1, tag) & 0xffff)) == 0)
  if (__builtin_expect(!GATHER_OK(), 0)) {
    unsigned spins = 0;
#pragma unroll 1
    do {
      __builtin_amdgcn_s_sleep(2);
      reload2(g0, g1, rws, go0, go1);
    } while (!GATHER_OK() && ++spins < SPIN_CAP);
  }
  float* const red2p = lds + L_RED2 + w * 128 + 8 * n16 + 4 * (g & 1);
  {
    const f32x2 q = sum_rows2(f32x2{asf(g0.x), asf(g0.z)}) + f32x2{asf(g1.x), asf(g1.z)};
    if (lane < 16) {
      if (w < 6) ((unsigned*)(lds + L_Q))[w * 16 + lane] = pk(q.x, q.y);
      else *(f32x2*)(lds + L_GU + (w - 6) * 32 + 2 * lane) = q;
    }
    if (w == 7) {
      const bool have = g != 0 && tagged(g1, tag);
      int e = have ? (int)(tref - g1.x) : 0;
      e = e < -400 ? -400 : (e > 400 ? 400 : e);
      const float cnt = wave_sum(have ? 1.f : 0.f), em = wave_sum((float)e) * __builtin_amdgcn_rcpf(fmaxf(cnt, 1.f));
      if (lane == 0) lds[L_DT] = em;
    }
  }
  wg_barrier();
  float sc[8];
  v4u qk[3];
#pragma unroll
  for (int t = 0; t < 3; ++t) qk[t] = *(const v4u*)(lds + L_Q + 32 * t + 4 * c8);
  tref += LEAD - (unsigned)__builtin_amdgcn_readfirstlane((int)lds[L_DT]);
  float wmax = -INFINITY;
#define QK(i) do { float d_[3]; \
    _Pragma("unroll") for (int t_ = 0; t_ < 3; ++t_) { d_[t_] = 0.f; _Pragma("unroll") for (int c_ = 0; c_ < 4; ++c_) d_[t_] = dot2(kv[3 * (i) + t_][c_], qk[t_][c_], d_[t_]); } \
    sc[i] = rb(rb(sum8((d_[0] + d_[1]) + d_[2])) * 0.07216878364870322f); wmax = fmaxf(wmax, sc[i]); PIN(); } while (0)
#define SLOT(n) not_before(tref + (((n) * TAU) >> 4))
  QK(0);
  pace<6>(); SLOT(8); ISSUE(8);
  QK(1); QK(2);
  unsigned hp;
  {
    const float ga = lds[L_GU + 4 * w + g], up = lds[L_GU + 32 + 4 * w + g];
    const float hv = rb(gelu_tanh(rb(ga))) * rb(up);
    hp = pk(hv, hv);
    asm volatile("" : "+v"(hp));
  }
  lockstep();
  pace<6>(); SLOT(9); ISSUE(9);
  QK(3); QK(4);
  lockstep();
  pace<6>(); SLOT(10); ISSUE(10);
  QK(5); QK(6); QK(7);
  wmax = max_u(wmax);
  if (lane == 0) lds[L_WM + w] = wmax;
  wg_barrier();
  float M;
  {
    const f32x4 m0 = *(const f32x4*)(lds + L_WM), m1 = *(const f32x4*)(lds + L_WM + 4);
    M = fmaxf(fmaxf(fmaxf(m0[0], m0[1]), fmaxf(m0[2], m0[3])), fmaxf(fmaxf(m1[0], m1[1]), fmaxf(m1[2], m1[3])));
  }
  constexpr float LOG2E = 1.4426950408889634f;
  float lsum = 0.f;
#pragma unroll
  for (int i = 0; i < 8; ++i) { sc[i] = __builtin_amdgcn_exp2f((sc[i] - M) * LOG2E); lsum += sc[i]; }
  lsum = sum_u(lsum);
  if (lane == 0) lds[L_WL + w] = lsum;
  f32x4 acc[3] = {f32x4{0.f, 0.f, 0.f, 0.f}, f32x4{0.f, 0.f, 0.f, 0.f}, f32x4{0.f, 0.f, 0.f, 0.f}};
#define PV(i) do { const v4u dp_ = diag(sc[i], mask); \
    _Pragma("unroll") for (int t_ = 0; t_ < 3; ++t_) acc[t_] = mfma(dp_, kv[24 + 3 * (i) + t_], acc[t_]); PIN(); } while (0)
  pace<6>(); SLOT(11); ISSUE(11); PV(0);
  wg_barrier();
  pace<6>(); SLOT(12); ISSUE(12);
  {
    const f32x4 l0 = *(const f32x4*)(lds + L_WL), l1 = *(const f32x4*)(lds + L_WL + 4);
    const float L = ((l0[0] + l0[1]) + (l0[2] + l0[3])) + ((l1[0] + l1[1]) + (l1[2] + l1[3]));
    publish(rws, tid == 384 ? WS_ML + (h * 8 + s) * 16 : OOB, f32x2{M, L}, tag);
    PIN();
  }
  PV(1); lockstep();
  pace<7>(); SLOT(13); ISSUE(13); PV(2); lockstep();
  pace<7>(); SLOT(14); ISSUE(14); PV(3); lockstep();
  pace<7>();
  const int mo = (w == 7 && lane < 8) ? WS_ML + (h * 8 + lane) * 16 : OOB;
  v4u ml = ld16<AUX_SC1>(rws, mo);
  PIN();
  SLOT(15); ISSUE(15); PV(4); lockstep();
  const int gd = 128 * rgl + 16 * w, hl = (gd * 683) >> 17;
  int o0 = WS_OP + ((4 * xg + hl) * 8 + u) * P_STRIDE + ((gd - 192 * hl) / 2 + c8) * 16;
  int ost = lane < 16 ? ob + (16 * w + lane) * 16 : OOB, yst = WS_YP + yb + lane * 16;
  asm volatile("" : "+v"(o0), "+v"(ost), "+v"(yst));
  PV(5);
  SLOT(16);
  const v4u wdr = ld16<AUX_NT>(rd, (4 * w + g) * 1280 + n16 * 16);
  PIN();
  v4u wor[4];
#pragma unroll
  for (int r = 0; r < 4; ++r) wor[r] = ld16<AUX_NT>(ro, (16 * w + 8 * (g >> 1) + (g & 1)) * 1280 + n16 * 16, r * 2560);
  PIN();
  PV(6);
  if (w == 7) {
    if (__builtin_expect((missing(ml, tag) & 0xff) != 0, 0)) {
      unsigned spins = 0;
#pragma unroll 1
      do { reload1(ml, rws, mo); } while ((missing(ml, tag) & 0xff) != 0 && ++spins < SPIN_CAP);
    }
    const float ms = asf(ml.x), MM = max8(ms);
    const float scale = __builtin_amdgcn_exp2f((M - MM) * LOG2E) * __builtin_amdgcn_rcpf(sum8(__builtin_amdgcn_exp2f((ms - MM) * LOG2E) * asf(ml.z)));
    if (lane == 0) lds[L_SC] = scale;
  }
  PIN();
  PV(7);
  if (valid) {
#pragma unroll
    for (int t = 0; t < 3; ++t) *(f32x4*)(redp + 64 * t) = acc[t];
  }
  wg_barrier();
  if (w < 6) publish(rws, ost, SUM192() * lds[L_SC], tag);
  const float* const sum2p = lds + L_RED2 + (lane >> 5) * 512 + 4 * (lane & 31);
#define SUM128(o) ({ const f32x4 s_ = (*(const f32x4*)(sum2p + (o)) + *(const f32x4*)(sum2p + (o) + 128)) + (*(const f32x4*)(sum2p + (o) + 256) + *(const f32x4*)(sum2p + (o) + 384)); \
    const auto a_ = __builtin_amdgcn_permlane32_swap(asu(s_[0]), asu(s_[2]), false, false), b_ = __builtin_amdgcn_permlane32_swap(asu(s_[1]), asu(s_[3]), false, false); \
    f32x2{asf(a_[0]), asf(b_[0])} + f32x2{asf(a_[1]), asf(b_[1])}; })
  unsigned spins = 0;
  if (tile) {
    const f32x4 z = mfma(v4u{hp, hp, hp, hp} & mask, wdr, f32x4{0.f, 0.f, 0.f, 0.f});
    if (lane < 32) *(f32x4*)(red2p + 1024) = z;
    wg_barrier();
    if (w == 7) publish(rws, yst + (WS_ZP - WS_YP), SUM128(1024), tag);
    if (w != 6) pace<PWAIT>(); else pace<0>();
    v4u e0;
#pragma unroll 1
    for (;;) {
      reload1(e0, rws, o0);
      if (missing(e0, tag) == 0) break;
      if (++spins >= SPIN_CAP) break;
    }
    float t0 = asf(e0.x), t1 = asf(e0.z);
    t0 += dpp<ROR8>(t0); t1 += dpp<ROR8>(t1);
    const float ov = sum_rows_xy(t0, t1);
    unsigned op = pk(ov, ov);
    op = (unsigned)__builtin_amdgcn_update_dpp((int)op, (int)op, ROR4, 0xc, 0xf, false);
    f32x4 y = mfma(diag_lane<0>(op, mask), wor[0], f32x4{0.f, 0.f, 0.f, 0.f});
    y = mfma(diag_lane<1>(op, mask), wor[1], y);
    y = mfma(diag_lane<2>(op, mask), wor[2], y);
    y = mfma(diag_lane<3>(op, mask), wor[3], y);
    if (lane < 32) *(f32x4*)red2p = y;
    wg_barrier();
    if (w == 0) {
      publish(rws, yst, SUM128(0), tag);
    }
  } else if (w >= 3) {
    const int unit = 5 * (xg + 8 * (idx - 30)) + w - 3;
    const int slot = 4 * (unit & 15) + (lane & 3);
    const int col = 128 * (unit >> 4) + 4 * (slot & 31) + 2 * (slot >> 5) + (g & 1);
    const int fo = ((unit >> 4) * 48 + (lane >> 2)) * Y_STRIDE + slot * 16;
    float xo = bflo(__builtin_amdgcn_raw_buffer_load_b16(rx, col * 2, 0, 0));
    v4u f0, f1, f2;
#pragma unroll 1
    do {
      reload3(f0, f1, f2, rws, WS_ZP + fo, WS_ZP + fo + 16 * Y_STRIDE, WS_ZP + fo + 32 * Y_STRIDE);
    } while ((missing(f0, tag) | missing(f1, tag) | missing(f2, tag)) != 0 && ++spins < SPIN_CAP);
    const float zz = colsum3(f0, f1, f2);
    float gt = rb(zz / (1.f + __builtin_amdgcn_exp2f(-zz * LOG2E)));
    int oc = (lane < 32 && n16 < 4) ? col * 2 : OOB;
    int y0 = WS_YP + fo, y1 = y0 + 16 * Y_STRIDE, y2 = y0 + 32 * Y_STRIDE;
    asm volatile("" : "+v"(gt), "+v"(xo), "+v"(oc), "+v"(y0), "+v"(y1), "+v"(y2));
    const rsrc_t rout = mk(out, D * 2);
#pragma unroll 1
    for (;;) {
      reload3(f0, f1, f2, rws, y0, y1, y2);
      if ((missing(f0, tag) | missing(f1, tag) | missing(f2, tag)) == 0) break;
      if (++spins >= SPIN_CAP) break;
    }
    __builtin_amdgcn_raw_buffer_store_b16((u16)pk(xo + rb(colsum3(f0, f1, f2) * gt), 0.f), rout, oc, 0, 0);
    if (unit == 0) __builtin_amdgcn_raw_buffer_store_b32(tag, rws, lane == 0 ? WS_EPOCH : OOB, 0, AUX_SC1);
  }
}

extern "C" void kernel_launch(void* const* d_in, const int* in_sizes, int n_in, void* d_out, int out_size, void* d_ws, size_t ws_size,
                              hipStream_t stream) {
  fused_decode<<<256, 512, 0, stream>>>((const u16*)d_in[0], (const u16*)d_in[1], (const u16*)d_in[2], (const u16*)d_in[3],
                                        (const u16*)d_in[4], (const u16*)d_in[6], (const u16*)d_in[7], (const u16*)d_in[8],
                                        (u16*)d_out, (unsigned char*)d_ws);
}
```

```cpp
#include <hip/hip_runtime.h>
#include <hip/hip_bf16.h>

typedef unsigned short u16;
typedef unsigned v4u __attribute__((ext_vector_type(4)));
typedef unsigned v16u __attribute__((ext_vector_type(16)));
typedef float f32x4 __attribute__((ext_vector_type(4)));
typedef float f32x2 __attribute__((ext_vector_type(2)));
typedef __bf16 bf16x8 __attribute__((ext_vector_type(8)));
typedef __bf16 bf16x2 __attribute__((ext_vector_type(2)));
typedef __amdgpu_buffer_rsrc_t rsrc_t;
typedef unsigned long long u64;

constexpr int D = 640, NH = 32, DH = 192, SK = 32768, WIN = 4096, FF = 1536;
constexpr int AUX_NT = 2, AUX_SC1 = 16;
constexpr int OOB = 0x40000000;
constexpr unsigned SPIN_CAP = 1u << 16;

constexpr int WS_EPOCH = 0;
constexpr int P_STRIDE = 1664, Y_STRIDE = 1152;
constexpr int WS_T0 = 2048;
constexpr int WS_ML = 4096;
constexpr int WS_QP = 8192;
constexpr int WS_OP = WS_QP + 240 * P_STRIDE;
constexpr int WS_ZP = WS_OP + 256 * P_STRIDE;
constexpr int WS_YP = WS_ZP + 240 * Y_STRIDE;
constexpr int WS_END = WS_YP + 240 * Y_STRIDE;

#define TAU 1376
#define EARLY 150
#define LEAD -158
#define CODEPF 9400

__device__ __forceinline__ rsrc_t mk(const void* p, int bytes) { return __builtin_amdgcn_make_buffer_rsrc((void*)p, 0, bytes, 0x00020000); }
template <int AUX> __device__ __forceinline__ v4u ld16(rsrc_t r, int voff, int soff = 0) { return __builtin_amdgcn_raw_buffer_load_b128(r, voff, soff, AUX); }
__device__ __forceinline__ float asf(unsigned u) { return __builtin_bit_cast(float, u); }
__device__ __forceinline__ unsigned asu(float f) { return __builtin_bit_cast(unsigned, f); }
__device__ __forceinline__ void publish(rsrc_t r, int voff, f32x2 v, unsigned tag) {
  __builtin_amdgcn_raw_buffer_store_b128(v4u{asu(v.x), tag, asu(v.y), tag}, r, voff, 0, AUX_SC1);
}
__device__ __forceinline__ float bflo(unsigned u) { return asf(u << 16); }
__device__ __forceinline__ float bfhi(unsigned u) { return asf(u & 0xffff0000u); }
__device__ __forceinline__ unsigned pk(float lo, float hi) {
  bf16x2 r = {(__bf16)lo, (__bf16)hi};
  return __builtin_bit_cast(unsigned, r);
}
__device__ __forceinline__ float rb(float f) { return (float)(__bf16)f; }
__device__ __forceinline__ float dot2(unsigned a, unsigned b, float c) {
  return __builtin_amdgcn_fdot2_f32_bf16(__builtin_bit_cast(bf16x2, a), __builtin_bit_cast(bf16x2, b), c, false);
}
template <int CTRL> __device__ __forceinline__ float dpp(float x) {
  return asf((unsigned)__builtin_amdgcn_mov_dpp((int)asu(x), CTRL, 0xf, 0xf, true));
}
constexpr int XOR1 = 0xB1, XOR2 = 0x4E, HMIR = 0x141, ROR8 = 0x128, ROR4 = 0x124, BCAST = 0x150;
__device__ __forceinline__ float sum8(float x) { x += dpp<XOR1>(x); x += dpp<XOR2>(x); x += dpp<HMIR>(x); return x; }
__device__ __forceinline__ float max8(float x) { x = fmaxf(x, dpp<XOR1>(x)); x = fmaxf(x, dpp<XOR2>(x)); x = fmaxf(x, dpp<HMIR>(x)); return x; }
__device__ __forceinline__ float sum_rows(float x) {
  auto s = __builtin_amdgcn_permlane16_swap(asu(x), asu(x), false, false);
  x = asf(s[0]) + asf(s[1]);
  auto t = __builtin_amdgcn_permlane32_swap(asu(x), asu(x), false, false);
  return asf(t[0]) + asf(t[1]);
}
__device__ __forceinline__ float max_rows(float x) {
  auto s = __builtin_amdgcn_permlane16_swap(asu(x), asu(x), false, false);
  x = fmaxf(asf(s[0]), asf(s[1]));
  auto t = __builtin_amdgcn_permlane32_swap(asu(x), asu(x), false, false);
  return fmaxf(asf(t[0]), asf(t[1]));
}
__device__ __forceinline__ float sum_u(float x) { x += dpp<ROR8>(x); return sum_rows(x); }
__device__ __forceinline__ float max_u(float x) { x = fmaxf(x, dpp<ROR8>(x)); return max_rows(x); }
__device__ __forceinline__ float wave_sum(float x) { return sum_u(sum8(x)); }
__device__ __forceinline__ float sum_rows_xy(float x, float y) {
  auto s = __builtin_amdgcn_permlane16_swap(asu(x), asu(y), false, false);
  const float t = asf(s[0]) + asf(s[1]);
  auto r = __builtin_amdgcn_permlane32_swap(asu(t), asu(t), false, false);
  return asf(r[0]) + asf(r[1]);
}
__device__ __forceinline__ f32x2 sum_rows2(f32x2 v) {
  auto a = __builtin_amdgcn_permlane16_swap(asu(v.x), asu(v.x), false, false), b = __builtin_amdgcn_permlane16_swap(asu(v.y), asu(v.y), false, false);
  v = f32x2{asf(a[0]), asf(b[0])} + f32x2{asf(a[1]), asf(b[1])};
  auto c = __builtin_amdgcn_permlane32_swap(asu(v.x), asu(v.x), false, false), d = __builtin_amdgcn_permlane32_swap(asu(v.y), asu(v.y), false, false);
  return f32x2{asf(c[0]), asf(d[0])} + f32x2{asf(c[1]), asf(d[1])};
}
__device__ __forceinline__ v4u diag(float val, const v4u& mask) { const unsigned p = pk(val, val); return v4u{p, p, p, p} & mask; }
template <int L> __device__ __forceinline__ v4u diag_lane(unsigned p, const v4u& mask) {
  const unsigned d = (unsigned)__builtin_amdgcn_mov_dpp((int)p, BCAST + L, 0xf, 0xf, true);
  return v4u{d, d, d, d} & mask;
}
__device__ __forceinline__ f32x4 mfma(v4u a, v4u b, f32x4 c) {
  return __builtin_amdgcn_mfma_f32_16x16x32_bf16(__builtin_bit_cast(bf16x8, a), __builtin_bit_cast(bf16x8, b), c, 0, 0, 0);
}
#define PIN() __builtin_amdgcn_sched_barrier(0)
__device__ __forceinline__ void wg_barrier() {
  asm volatile("s_waitcnt lgkmcnt(0)" ::: "memory");
  __builtin_amdgcn_s_barrier();
  asm volatile("" ::: "memory");
}
__device__ __forceinline__ void lockstep() { PIN(); __builtin_amdgcn_s_barrier(); PIN(); }
template <int N> __device__ __forceinline__ void pace() { PIN(); asm volatile("s_waitcnt vmcnt(%0)" :: "i"(N)); PIN(); }
__device__ __forceinline__ unsigned clock_lo() {
  u64 t;
  asm volatile("s_memrealtime %0\n\ts_waitcnt lgkmcnt(0)" : "=s"(t) :: "memory");
  return (unsigned)t;
}
__device__ __forceinline__ void not_before(unsigned t) {
  PIN();
  asm volatile("1:\n\ts_memrealtime vcc\n\ts_waitcnt lgkmcnt(0)\n\ts_sub_u32 vcc_lo, vcc_lo, %0\n\ts_cmp_lt_i32 vcc_lo, 0\n\ts_cbranch_scc1 1b"
               :: "s"(t) : "vcc", "scc", "memory");
  PIN();
}
__device__ __forceinline__ bool tagged(const v4u& g, unsigned tag) { return (g.y == tag) & (g.w == tag); }
__device__ __forceinline__ u64 missing(const v4u& g, unsigned tag) { return __builtin_amdgcn_ballot_w64(g.y != tag) | __builtin_amdgcn_ballot_w64(g.w != tag); }
__device__ __forceinline__ void reload1(v4u& a, rsrc_t rs, int oa) {
  asm volatile("buffer_load_dwordx4 %0, %1, %2, 0 offen sc1\n\ts_waitcnt vmcnt(0)" : "=&v"(a) : "v"(oa), "s"(rs) : "memory");
}
__device__ __forceinline__ void reload2(v4u& a, v4u& b, rsrc_t rs, int oa, int ob) {
  asm volatile("buffer_load_dwordx4 %0, %2, %4, 0 offen sc1\n\tbuffer_load_dwordx4 %1, %3, %4, 0 offen sc1\n\ts_waitcnt vmcnt(0)"
               : "=&v"(a), "=&v"(b) : "v"(oa), "v"(ob), "s"(rs) : "memory");
}
__device__ __forceinline__ void reload3(v4u& a, v4u& b, v4u& c, rsrc_t rs, int oa, int ob, int oc) {
  asm volatile("buffer_load_dwordx4 %0, %3, %6, 0 offen sc1\n\tbuffer_load_dwordx4 %1, %4, %6, 0 offen sc1\n\t"
               "buffer_load_dwordx4 %2, %5, %6, 0 offen sc1\n\ts_waitcnt vmcnt(0)"
               : "=&v"(a), "=&v"(b), "=&v"(c) : "v"(oa), "v"(ob), "v"(oc), "s"(rs) : "memory");
}
__device__ __forceinline__ float gelu_tanh(float x) {
  const float u = 0.7978845608028654f * (x + 0.044715f * x * x * x);
  const float e = __builtin_amdgcn_exp2f(u * 2.885390081777927f);
  const float th = 1.f - 2.f / (e + 1.f);
  return 0.5f * x * (1.f + th);
}
__device__ __forceinline__ float colsum3(const v4u& a, const v4u& b, const v4u& c) {
  float t0 = (asf(a.x) + asf(b.x)) + asf(c.x), t1 = (asf(a.z) + asf(b.z)) + asf(c.z);
  t0 += dpp<ROR4>(t0); t1 += dpp<ROR4>(t1);
  t0 += dpp<ROR8>(t0); t1 += dpp<ROR8>(t1);
  return rb(sum_rows_xy(t0, t1));
}

constexpr int L_RED = 0;
constexpr int L_RED2 = 16 * 192;
constexpr int L_Q = L_RED2 + 16 * 128;
constexpr int L_GU = L_Q + 96;
constexpr int L_WM = L_GU + 64;
constexpr int L_WL = L_WM + 8;
constexpr int L_DT = L_WL + 8;
constexpr int L_SC = L_DT + 1;
constexpr int L_END = L_SC + 3;

__global__ void __launch_bounds__(512, 2)
fused_decode(const u16* x_, const u16* kc_, const u16* vc_, const u16* wq_, const u16* wo_, const u16* wg_, const u16* wu_,
             const u16* wd_, u16* out_, unsigned char* ws_) {
  __shared__ __attribute__((aligned(16))) float lds[L_END];
  const int b = blockIdx.x, xg = b & 7, idx = b >> 3, h = 4 * xg + (idx & 3), s = idx >> 2;
  const int tid = threadIdx.x, lane = tid & 63, w = __builtin_amdgcn_readfirstlane(tid >> 6);
  const int u = lane >> 3, c8 = lane & 7, g = lane >> 4, n16 = lane & 15;
  v16u ka; v4u kb; u64 ts64;
  asm volatile("s_load_dwordx16 %0, %3, 0x0\n\ts_load_dwordx4 %1, %3, 0x40\n\ts_memrealtime %2"
               : "=&s"(ka), "=&s"(kb), "=&s"(ts64) : "s"(__builtin_amdgcn_kernarg_segment_ptr()));
  PIN();
  unsigned cpf0 = 0, cpf1 = 0;
  if (w == 0 && idx >= 30) {
    u64 pc;
    asm volatile("s_getpc_b64 %0" : "=s"(pc));
    const rsrc_t rc = mk((const void*)pc, CODEPF);
    asm volatile("buffer_load_dword %0, %2, %3, 0 offen\n\tbuffer_load_dword %1, %2, %3, %4 offen"
                 : "+v"(cpf0), "+v"(cpf1) : "v"(lane * 128), "s"(rc), "s"(8192));
  }
  PIN();
  const int rgl = idx / 5, cb = idx - 5 * rgl;
  const bool tile = idx < 30;
  int astride = rgl < 4 ? NH * DH * 2 : FF * 2;
  int aoff = 128 * cb * astride + (rgl < 4 ? 1536 * xg + 384 * rgl : 384 * xg);
  int abytes = tile ? 127 * astride + 384 : 0;
  int asel = rgl < 4 ? 0 : rgl - 3;
  int xro = (128 * cb + 16 * w + 2 * u) * 2;
  int ao = (16 * w + 2 * u) * astride + c8 * 16;
  unsigned kvoff = ((unsigned)h * SK + (SK - WIN)) * (DH * 2);
  int kvo = u * 384 + c8 * 16, kvs = w * 3072 + s * 24576;
  asm volatile("" : "+s"(astride), "+s"(aoff), "+s"(abytes), "+s"(asel), "+s"(kvoff), "+s"(kvs), "+v"(xro), "+v"(ao), "+v"(kvo));
  PIN();
  asm volatile("s_waitcnt lgkmcnt(0)" : "+s"(ka), "+s"(kb), "+s"(ts64));
  const unsigned ts = (unsigned)ts64 + EARLY;
#define SLOT0(n) not_before(ts + (((n) * TAU) >> 4))
#define KARG(lo, hi) (((u64)(hi) << 32) | (lo))
  const u16 *x = (const u16*)KARG(ka[0], ka[1]), *kc = (const u16*)KARG(ka[2], ka[3]), *vc = (const u16*)KARG(ka[4], ka[5]);
  const u16 *wq = (const u16*)KARG(ka[6], ka[7]), *wo = (const u16*)KARG(ka[8], ka[9]), *wg = (const u16*)KARG(ka[10], ka[11]);
  const u16 *wu = (const u16*)KARG(ka[12], ka[13]), *wd = (const u16*)KARG(ka[14], ka[15]);
  u16* out = (u16*)KARG(kb[0], kb[1]);
  unsigned char* ws = (unsigned char*)KARG(kb[2], kb[3]);

  const rsrc_t ra = mk((const char*)(asel == 0 ? wq : (asel == 1 ? wg : wu)) + aoff, abytes);
  v4u a[6];
#pragma unroll
  for (int t = 0; t < 3; ++t) a[t] = ld16<AUX_NT>(ra, ao + t * 128);
  PIN();
  const rsrc_t rx = mk(x, D * 2);
  const v4u xs = ld16<0>(rx, lane * 16);
  const unsigned xs4 = __builtin_amdgcn_raw_buffer_load_b32(rx, lane * 4 + 1024, 0, 0);
  const unsigned xr = __builtin_amdgcn_raw_buffer_load_b32(rx, xro, 0, 0);
  PIN();
#pragma unroll
  for (int t = 0; t < 3; ++t) a[3 + t] = ld16<AUX_NT>(ra, ao + t * 128, astride);
  PIN();
  const rsrc_t rws = mk(ws, WS_END);
  const unsigned epoch = __builtin_amdgcn_raw_buffer_load_b32(rws, WS_EPOCH, 0, AUX_SC1);
  const rsrc_t rk = mk((const char*)kc + kvoff, WIN * DH * 2), rv = mk((const char*)vc + kvoff, WIN * DH * 2);
  lockstep();
  v4u kv[48];
#define ISSUE(n) do { _Pragma("unroll") for (int t_ = 0; t_ < 3; ++t_) { \
    kv[3 * (n) + t_] = ld16<AUX_NT>((n) < 8 ? rk : rv, kvo + t_ * 128, kvs + (((n) + h) & 7) * 196608); PIN(); } } while (0)
  ISSUE(0);
  v4u mask;
  {
    const unsigned hm = (lane & 1) ? 0xffff0000u : 0x0000ffffu;
    const int r = c8 >> 1;
    mask = v4u{r == 0 ? hm : 0u, r == 1 ? hm : 0u, r == 2 ? hm : 0u, r == 3 ? hm : 0u};
  }
  pace<10>();
  ISSUE(1);
  float xn0, xn1;
  {
    const unsigned xv[5] = {xs.x, xs.y, xs.z, xs.w, xs4};
    float sum = 0.f;
#pragma unroll
    for (int j = 0; j < 5; ++j) sum += bflo(xv[j]) + bfhi(xv[j]);
    const float mean = wave_sum(sum) * (1.f / D);
    float sq = 0.f;
#pragma unroll
    for (int j = 0; j < 5; ++j) { const float d0 = bflo(xv[j]) - mean, d1 = bfhi(xv[j]) - mean; sq += d0 * d0 + d1 * d1; }
    const float var = rb(wave_sum(sq) * (1.f / D));
    const float mb = rb(mean), sd = rb(sqrtf(rb(var + 1e-5f)));
    xn0 = rb(bflo(xr) - mb) / sd;
    xn1 = rb(bfhi(xr) - mb) / sd;
  }
  asm volatile("" : "+v"(xn0) : "v"(cpf0), "v"(cpf1));
  const bool valid = ((lane >> 3) & 1) == (lane >> 5);
  float* const redp = lds + L_RED + (w * 2 + (lane >> 5)) * 192 + 8 * c8 + 4 * (g & 1);
  {
    const v4u d0 = diag(xn0, mask), d1 = diag(xn1, mask);
    f32x4 acc[3];
#pragma unroll
    for (int t = 0; t < 3; ++t) acc[t] = mfma(d0, a[t], f32x4{0.f, 0.f, 0.f, 0.f});
#pragma unroll
    for (int t = 0; t < 3; ++t) acc[t] = mfma(d1, a[3 + t], acc[t]);
    if (valid) {
#pragma unroll
      for (int t = 0; t < 3; ++t) *(f32x4*)(redp + 64 * t) = acc[t];
    }
    PIN();
  }
  ISSUE(2);
  const unsigned tag = (unsigned)__builtin_amdgcn_readfirstlane((int)epoch) * 0x9E3779B1u + 0x7F4A7C15u;
  wg_barrier();
  const float* const sump = lds + L_RED + 4 * g * 192 + 32 * w + 2 * n16;
#define SUM192() sum_rows2((*(const f32x2*)sump + *(const f32x2*)(sump + 192)) + (*(const f32x2*)(sump + 384) + *(const f32x2*)(sump + 576)))
  unsigned tref;
  {
    f32x2 sum = SUM192();
    tref = clock_lo();
    if (w == 6) sum.x = asf(tref);
    publish(rws, !tile ? OOB : ((w < 6 && lane < 16) ? WS_QP + (xg * 30 + idx) * P_STRIDE + (16 * w + lane) * 16
                                                     : ((tid == 384 && cb == 2) ? WS_T0 + (rgl * 8 + xg) * 16 : OOB)), sum, tag);
  }
  PIN();
  const int yb = (cb * 48 + 6 * xg + rgl) * Y_STRIDE;
  const int ob = WS_OP + (h * 8 + s) * P_STRIDE;
  const rsrc_t rd = mk(wd + (size_t)(32 * (6 * xg + rgl)) * D + 128 * cb, tile ? 31 * D * 2 + 256 : 0);
  const rsrc_t ro = mk(wo + (size_t)(128 * (6 * xg + rgl)) * D + 128 * cb, tile ? 127 * D * 2 + 256 : 0);
  const bool glive = w < 6 || tile;
  const int gb = WS_QP + (xg * 30 + 5 * (w < 6 ? (idx & 3) : w - 2)) * P_STRIDE + (16 * (w < 6 ? w : rgl) + n16) * 16;
  const int go0 = glive ? gb + g * P_STRIDE : OOB;
  const int wl = 48 * w + lane - 16;
  const int go1 = g == 0 ? (glive ? gb + 4 * P_STRIDE : OOB)
                         : (wl < 96 ? ob + wl * 16
                                    : (wl < 224 ? (tile ? (wl < 160 ? WS_YP - 96 * 16 : WS_ZP - 160 * 16) + yb + wl * 16 : OOB)
                                                : (w == 7 ? WS_T0 + (lane - 16) * 16 : OOB)));
  pace<7>(); SLOT0(3); ISSUE(3); lockstep();
  pace<7>(); SLOT0(4); ISSUE(4); lockstep();
  pace<7>(); SLOT0(5);
  v4u g0 = ld16<AUX_SC1>(rws, go0), g1 = ld16<AUX_SC1>(rws, go1);
  lockstep();
  ISSUE(5); lockstep();
  pace<8>(); SLOT0(6); ISSUE(6); lockstep();
  pace<6>(); SLOT0(7); ISSUE(7);
#define GATHER_OK() (!glive || (missing(g0, tag) | (missing(g1, tag) & 0xffff)) == 0)
  if (__builtin_expect(!GATHER_OK(), 0)) {
    unsigned spins = 0;
#pragma unroll 1
    do {
      __builtin_amdgcn_s_sleep(2);
      reload2(g0, g1, rws, go0, go1);
    } while (!GATHER_OK() && ++spins < SPIN_CAP);
  }
  float* const red2p = lds + L_RED2 + w * 128 + 8 * n16 + 4 * (g & 1);
  {
    const f32x2 q = sum_rows2(f32x2{asf(g0.x), asf(g0.z)}) + f32x2{asf(g1.x), asf(g1.z)};
    if (lane < 16) {
      if (w < 6) ((unsigned*)(lds + L_Q))[w * 16 + lane] = pk(q.x, q.y);
      else *(f32x2*)(lds + L_GU + (w - 6) * 32 + 2 * lane) = q;
    }
    if (w == 7) {
      const bool have = g != 0 && tagged(g1, tag);
      int e = have ? (int)(tref - g1.x) : 0;
      e = e < -400 ? -400 : (e > 400 ? 400 : e);
      const float cnt = wave_sum(have ? 1.f : 0.f), em = wave_sum((float)e) * __builtin_amdgcn_rcpf(fmaxf(cnt, 1.f));
      if (lane == 0) lds[L_DT] = em;
    }
  }
  wg_barrier();
  float sc[8];
  v4u qk[3];
#pragma unroll
  for (int t = 0; t < 3; ++t) qk[t] = *(const v4u*)(lds + L_Q + 32 * t + 4 * c8);
  tref += LEAD - (unsigned)__builtin_amdgcn_readfirstlane((int)lds[L_DT]);
  float wmax = -INFINITY;
#define QK(i) do { float d_[3]; \
    _Pragma("unroll") for (int t_ = 0; t_ < 3; ++t_) { d_[t_] = 0.f; _Pragma("unroll") for (int c_ = 0; c_ < 4; ++c_) d_[t_] = dot2(kv[3 * (i) + t_][c_], qk[t_][c_], d_[t_]); } \
    sc[i] = rb(rb(sum8((d_[0] + d_[1]) + d_[2])) * 0.07216878364870322f); wmax = fmaxf(wmax, sc[i]); PIN(); } while (0)
#define SLOT(n) not_before(tref + (((n) * TAU) >> 4))
  QK(0);
  pace<6>(); SLOT(8); ISSUE(8);
  QK(1); QK(2);
  unsigned hp;
  {
    const float ga = lds[L_GU + 4 * w + g], up = lds[L_GU + 32 + 4 * w + g];
    const float hv = rb(gelu_tanh(rb(ga))) * rb(up);
    hp = pk(hv, hv);
    asm volatile("" : "+v"(hp));
  }
  lockstep();
  pace<6>(); SLOT(9); ISSUE(9);
  QK(3); QK(4);
  lockstep();
  pace<6>(); SLOT(10); ISSUE(10);
  QK(5); QK(6); QK(7);
  wmax = max_u(wmax);
  if (lane == 0) lds[L_WM + w] = wmax;
  wg_barrier();
  float M;
  {
    const f32x4 m0 = *(const f32x4*)(lds + L_WM), m1 = *(const f32x4*)(lds + L_WM + 4);
    M = fmaxf(fmaxf(fmaxf(m0[0], m0[1]), fmaxf(m0[2], m0[3])), fmaxf(fmaxf(m1[0], m1[1]), fmaxf(m1[2], m1[3])));
  }
  constexpr float LOG2E = 1.4426950408889634f;
  float lsum = 0.f;
#pragma unroll
  for (int i = 0; i < 8; ++i) { sc[i] = __builtin_amdgcn_exp2f((sc[i] - M) * LOG2E); lsum += sc[i]; }
  lsum = sum_u(lsum);
  if (lane == 0) lds[L_WL + w] = lsum;
  f32x4 acc[3] = {f32x4{0.f, 0.f, 0.f, 0.f}, f32x4{0.f, 0.f, 0.f, 0.f}, f32x4{0.f, 0.f, 0.f, 0.f}};
#define PV(i) do { const v4u dp_ = diag(sc[i], mask); \
    _Pragma("unroll") for (int t_ = 0; t_ < 3; ++t_) acc[t_] = mfma(dp_, kv[24 + 3 * (i) + t_], acc[t_]); PIN(); } while (0)
  pace<6>(); SLOT(11); ISSUE(11); PV(0);
  wg_barrier();
  pace<6>(); SLOT(12); ISSUE(12);
  {
    const f32x4 l0 = *(const f32x4*)(lds + L_WL), l1 = *(const f32x4*)(lds + L_WL + 4);
    const float L = ((l0[0] + l0[1]) + (l0[2] + l0[3])) + ((l1[0] + l1[1]) + (l1[2] + l1[3]));
    publish(rws, tid == 384 ? WS_ML + (h * 8 + s) * 16 : OOB, f32x2{M, L}, tag);
    PIN();
  }
  PV(1); lockstep();
  pace<7>(); SLOT(13); ISSUE(13); PV(2); lockstep();
  pace<7>(); SLOT(14); ISSUE(14); PV(3); lockstep();
  pace<7>();
  const int mo = (w == 7 && lane < 8) ? WS_ML + (h * 8 + lane) * 16 : OOB;
  v4u ml = ld16<AUX_SC1>(rws, mo);
  PIN();
  SLOT(15); ISSUE(15); PV(4); lockstep();
  const int gd = 128 * rgl + 16 * w, hl = (gd * 683) >> 17;
  int o0 = WS_OP + ((4 * xg + hl) * 8 + u) * P_STRIDE + ((gd - 192 * hl) / 2 + c8) * 16;
  int ost = lane < 16 ? ob + (16 * w + lane) * 16 : OOB, yst = WS_YP + yb + lane * 16;
  asm volatile("" : "+v"(o0), "+v"(ost), "+v"(yst));
  PV(5);
  SLOT(16);
  const v4u wdr = ld16<AUX_NT>(rd, (4 * w + g) * 1280 + n16 * 16);
  PIN();
  v4u wor[4];
#pragma unroll
  for (int r = 0; r < 4; ++r) wor[r] = ld16<AUX_NT>(ro, (16 * w + 8 * (g >> 1) + (g & 1)) * 1280 + n16 * 16, r * 2560);
  PIN();
  PV(6);
  if (w == 7) {
    if (__builtin_expect((missing(ml, tag) & 0xff) != 0, 0)) {
      unsigned spins = 0;
#pragma unroll 1
      do { reload1(ml, rws, mo); } while ((missing(ml, tag) & 0xff) != 0 && ++spins < SPIN_CAP);
    }
    const float ms = asf(ml.x), MM = max8(ms);
    const float scale = __builtin_amdgcn_exp2f((M - MM) * LOG2E) * __builtin_amdgcn_rcpf(sum8(__builtin_amdgcn_exp2f((ms - MM) * LOG2E) * asf(ml.z)));
    if (lane == 0) lds[L_SC] = scale;
  }
  PIN();
  PV(7);
  if (valid) {
#pragma unroll
    for (int t = 0; t < 3; ++t) *(f32x4*)(redp + 64 * t) = acc[t];
  }
  wg_barrier();
  if (w < 6) publish(rws, ost, SUM192() * lds[L_SC], tag);
  const float* const sum2p = lds + L_RED2 + (lane >> 5) * 512 + 4 * (lane & 31);
#define SUM128(o) ({ const f32x4 s_ = (*(const f32x4*)(sum2p + (o)) + *(const f32x4*)(sum2p + (o) + 128)) + (*(const f32x4*)(sum2p + (o) + 256) + *(const f32x4*)(sum2p + (o) + 384)); \
    const auto a_ = __builtin_amdgcn_permlane32_swap(asu(s_[0]), asu(s_[2]), false, false), b_ = __builtin_amdgcn_permlane32_swap(asu(s_[1]), asu(s_[3]), false, false); \
    f32x2{asf(a_[0]), asf(b_[0])} + f32x2{asf(a_[1]), asf(b_[1])}; })
  unsigned spins = 0;
  if (tile) {
    const f32x4 z = mfma(v4u{hp, hp, hp, hp} & mask, wdr, f32x4{0.f, 0.f, 0.f, 0.f});
    if (lane < 32) *(f32x4*)(red2p + 1024) = z;
    wg_barrier();
    if (w == 7) publish(rws, yst + (WS_ZP - WS_YP), SUM128(1024), tag);
    if (w != 6) pace<1>(); else pace<0>();
    v4u e0;
#pragma unroll 1
    for (;;) {
      reload1(e0, rws, o0);
      if (missing(e0, tag) == 0) break;
      if (++spins >= SPIN_CAP) break;
    }
    float t0 = asf(e0.x), t1 = asf(e0.z);
    t0 += dpp<ROR8>(t0); t1 += dpp<ROR8>(t1);
    const float ov = sum_rows_xy(t0, t1);
    unsigned op = pk(ov, ov);
    op = (unsigned)__builtin_amdgcn_update_dpp((int)op, (int)op, ROR4, 0xc, 0xf, false);
    f32x4 y = mfma(diag_lane<0>(op, mask), wor[0], f32x4{0.f, 0.f, 0.f, 0.f});
    y = mfma(diag_lane<1>(op, mask), wor[1], y);
    y = mfma(diag_lane<2>(op, mask), wor[2], y);
    y = mfma(diag_lane<3>(op, mask), wor[3], y);
    if (lane < 32) *(f32x4*)red2p = y;
    wg_barrier();
    if (w == 0) publish(rws, yst, SUM128(0), tag);
  } else if (w >= 3) {
    const int unit = 5 * (xg + 8 * (idx - 30)) + w - 3;
    const int slot = 4 * (unit & 15) + (lane & 3);
    const int col = 128 * (unit >> 4) + 4 * (slot & 31) + 2 * (slot >> 5) + (g & 1);
    const int fo = ((unit >> 4) * 48 + (lane >> 2)) * Y_STRIDE + slot * 16;
    float xo = bflo(__builtin_amdgcn_raw_buffer_load_b16(rx, col * 2, 0, 0));
    v4u f0, f1, f2;
#pragma unroll 1
    do {
      reload3(f0, f1, f2, rws, WS_ZP + fo, WS_ZP + fo + 16 * Y_STRIDE, WS_ZP + fo + 32 * Y_STRIDE);
    } while ((missing(f0, tag) | missing(f1, tag) | missing(f2, tag)) != 0 && ++spins < SPIN_CAP);
    const float zz = colsum3(f0, f1, f2);
    float gt = rb(zz / (1.f + __builtin_amdgcn_exp2f(-zz * LOG2E)));
    int oc = (lane < 32 && n16 < 4) ? col * 2 : OOB;
    int y0 = WS_YP + fo, y1 = y0 + 16 * Y_STRIDE, y2 = y0 + 32 * Y_STRIDE;
    asm volatile("" : "+v"(gt), "+v"(xo), "+v"(oc), "+v"(y0), "+v"(y1), "+v"(y2));
    const rsrc_t rout = mk(out, D * 2);
#pragma unroll 1
    for (;;) {
      reload3(f0, f1, f2, rws, y0, y1, y2);
      if ((missing(f0, tag) | missing(f1, tag) | missing(f2, tag)) == 0) break;
      if (++spins >= SPIN_CAP) break;
    }
    __builtin_amdgcn_raw_buffer_store_b16((u16)pk(xo + rb(colsum3(f0, f1, f2) * gt), 0.f), rout, oc, 0, 0);
    if (unit == 0) __builtin_amdgcn_raw_buffer_store_b32(tag, rws, lane == 0 ? WS_EPOCH : OOB, 0, AUX_SC1);
  }
}

extern "C" void kernel_launch(void* const* d_in, const int* in_sizes, int n_in, void* d_out, int out_size, void* d_ws, size_t ws_size,
                              hipStream_t stream) {
  fused_decode<<<256, 512, 0, stream>>>((const u16*)d_in[0], (const u16*)d_in[1], (const u16*)d_in[2], (const u16*)d_in[3],
                                        (const u16*)d_in[4], (const u16*)d_in[6], (const u16*)d_in[7], (const u16*)d_in[8],
                                        (u16*)d_out, (unsigned char*)d_ws);
}
```

```cpp
#include <hip/hip_runtime.h>
#include <hip/hip_bf16.h>

typedef unsigned short u16;
typedef unsigned v4u __attribute__((ext_vector_type(4)));
typedef unsigned v16u __attribute__((ext_vector_type(16)));
typedef float f32x4 __attribute__((ext_vector_type(4)));
typedef float f32x2 __attribute__((ext_vector_type(2)));
typedef __bf16 bf16x8 __attribute__((ext_vector_type(8)));
typedef __bf16 bf16x2 __attribute__((ext_vector_type(2)));
typedef __amdgpu_buffer_rsrc_t rsrc_t;
typedef unsigned long long u64;

constexpr int D = 640, NH = 32, DH = 192, SK = 32768, WIN = 4096, FF = 1536;
constexpr int AUX_NT = 2, AUX_SC1 = 16;
constexpr int OOB = 0x40000000;
constexpr unsigned SPIN_CAP = 1u << 16;

constexpr int WS_EPOCH = 0;
constexpr int P_STRIDE = 1664, Y_STRIDE = 1152;
constexpr int WS_T0 = 2048;
constexpr int WS_ML = 4096;
constexpr int WS_QP = 8192;
constexpr int WS_OP = WS_QP + 240 * P_STRIDE;
constexpr int WS_ZP = WS_OP + 256 * P_STRIDE;
constexpr int WS_YP = WS_ZP + 240 * Y_STRIDE;
constexpr int WS_END = WS_YP + 240 * Y_STRIDE;

#define TAU 1390
#define EARLY 165
#define LEAD -160
#define CODEPF 9300

__device__ __forceinline__ rsrc_t mk(const void* p, int bytes) { return __builtin_amdgcn_make_buffer_rsrc((void*)p, 0, bytes, 0x00020000); }
template <int AUX> __device__ __forceinline__ v4u ld16(rsrc_t r, int voff, int soff = 0) { return __builtin_amdgcn_raw_buffer_load_b128(r, voff, soff, AUX); }
__device__ __forceinline__ float asf(unsigned u) { return __builtin_bit_cast(float, u); }
__device__ __forceinline__ unsigned asu(float f) { return __builtin_bit_cast(unsigned, f); }
__device__ __forceinline__ void publish(rsrc_t r, int voff, f32x2 v, unsigned tag) {
  __builtin_amdgcn_raw_buffer_store_b128(v4u{asu(v.x), tag, asu(v.y), tag}, r, voff, 0, AUX_SC1);
}
__device__ __forceinline__ float bflo(unsigned u) { return asf(u << 16); }
__device__ __forceinline__ float bfhi(unsigned u) { return asf(u & 0xffff0000u); }
__device__ __forceinline__ unsigned pk(float lo, float hi) {
  bf16x2 r = {(__bf16)lo, (__bf16)hi};
  return __builtin_bit_cast(unsigned, r);
}
__device__ __forceinline__ float rb(float f) { return (float)(__bf16)f; }
__device__ __forceinline__ float dot2(unsigned a, unsigned b, float c) {
  return __builtin_amdgcn_fdot2_f32_bf16(__builtin_bit_cast(bf16x2, a), __builtin_bit_cast(bf16x2, b), c, false);
}
template <int CTRL> __device__ __forceinline__ float dpp(float x) {
  return asf((unsigned)__builtin_amdgcn_mov_dpp((int)asu(x), CTRL, 0xf, 0xf, true));
}
constexpr int XOR1 = 0xB1, XOR2 = 0x4E, HMIR = 0x141, ROR8 = 0x128, ROR4 = 0x124, BCAST = 0x150;
__device__ __forceinline__ float sum8(float x) { x += dpp<XOR1>(x); x += dpp<XOR2>(x); x += dpp<HMIR>(x); return x; }
__device__ __forceinline__ float max8(float x) { x = fmaxf(x, dpp<XOR1>(x)); x = fmaxf(x, dpp<XOR2>(x)); x = fmaxf(x, dpp<HMIR>(x)); return x; }
__device__ __forceinline__ float sum_rows(float x) {
  auto s = __builtin_amdgcn_permlane16_swap(asu(x), asu(x), false, false);
  x = asf(s[0]) + asf(s[1]);
  auto t = __builtin_amdgcn_permlane32_swap(asu(x), asu(x), false, false);
  return asf(t[0]) + asf(t[1]);
}
__device__ __forceinline__ float max_rows(float x) {
  auto s = __builtin_amdgcn_permlane16_swap(asu(x), asu(x), false, false);
  x = fmaxf(asf(s[0]), asf(s[1]));
  auto t = __builtin_amdgcn_permlane32_swap(asu(x), asu(x), false, false);
  return fmaxf(asf(t[0]), asf(t[1]));
}
__device__ __forceinline__ float sum_u(float x) { x += dpp<ROR8>(x); return sum_rows(x); }
__device__ __forceinline__ float max_u(float x) { x = fmaxf(x, dpp<ROR8>(x)); return max_rows(x); }
__device__ __forceinline__ float wave_sum(float x) { return sum_u(sum8(x)); }
__device__ __forceinline__ float sum_rows_xy(float x, float y) {
  auto s = __builtin_amdgcn_permlane16_swap(asu(x), asu(y), false, false);
  const float t = asf(s[0]) + asf(s[1]);
  auto r = __builtin_amdgcn_permlane32_swap(asu(t), asu(t), false, false);
  return asf(r[0]) + asf(r[1]);
}
__device__ __forceinline__ f32x2 sum_rows2(f32x2 v) {
  auto a = __builtin_amdgcn_permlane16_swap(asu(v.x), asu(v.x), false, false), b = __builtin_amdgcn_permlane16_swap(asu(v.y), asu(v.y), false, false);
  v = f32x2{asf(a[0]), asf(b[0])} + f32x2{asf(a[1]), asf(b[1])};
  auto c = __builtin_amdgcn_permlane32_swap(asu(v.x), asu(v.x), false, false), d = __builtin_amdgcn_permlane32_swap(asu(v.y), asu(v.y), false, false);
  return f32x2{asf(c[0]), asf(d[0])} + f32x2{asf(c[1]), asf(d[1])};
}
__device__ __forceinline__ v4u diag(float val, const v4u& mask) { const unsigned p = pk(val, val); return v4u{p, p, p, p} & mask; }
template <int L> __device__ __forceinline__ v4u diag_lane(unsigned p, const v4u& mask) {
  const unsigned d = (unsigned)__builtin_amdgcn_mov_dpp((int)p, BCAST + L, 0xf, 0xf, true);
  return v4u{d, d, d, d} & mask;
}
__device__ __forceinline__ f32x4 mfma(v4u a, v4u b, f32x4 c) {
  return __builtin_amdgcn_mfma_f32_16x16x32_bf16(__builtin_bit_cast(bf16x8, a), __builtin_bit_cast(bf16x8, b), c, 0, 0, 0);
}
#define PIN() __builtin_amdgcn_sched_barrier(0)
__device__ __forceinline__ void wg_barrier() {
  asm volatile("s_waitcnt lgkmcnt(0)" ::: "memory");
  __builtin_amdgcn_s_barrier();
  asm volatile("" ::: "memory");
}
__device__ __forceinline__ void lockstep() { PIN(); __builtin_amdgcn_s_barrier(); PIN(); }
template <int N> __device__ __forceinline__ void pace() { PIN(); asm volatile("s_waitcnt vmcnt(%0)" :: "i"(N)); PIN(); }
__device__ __forceinline__ unsigned clock_lo() {
  u64 t;
  asm volatile("s_memrealtime %0\n\ts_waitcnt lgkmcnt(0)" : "=s"(t) :: "memory");
  return (unsigned)t;
}
__device__ __forceinline__ void not_before(unsigned t) {
  PIN();
  asm volatile("1:\n\ts_memrealtime vcc\n\ts_waitcnt lgkmcnt(0)\n\ts_sub_u32 vcc_lo, vcc_lo, %0\n\ts_cmp_lt_i32 vcc_lo, 0\n\ts_cbranch_scc1 1b"
               :: "s"(t) : "vcc", "scc", "memory");
  PIN();
}
__device__ __forceinline__ bool tagged(const v4u& g, unsigned tag) { return (g.y == tag) & (g.w == tag); }
__device__ __forceinline__ u64 missing(const v4u& g, unsigned tag) { return __builtin_amdgcn_ballot_w64(g.y != tag) | __builtin_amdgcn_ballot_w64(g.w != tag); }
__device__ __forceinline__ void reload1(v4u& a, rsrc_t rs, int oa) {
  asm volatile("buffer_load_dwordx4 %0, %1, %2, 0 offen sc1\n\ts_waitcnt vmcnt(0)" : "=&v"(a) : "v"(oa), "s"(rs) : "memory");
}
__device__ __forceinline__ void reload2(v4u& a, v4u& b, rsrc_t rs, int oa, int ob) {
  asm volatile("buffer_load_dwordx4 %0, %2, %4, 0 offen sc1\n\tbuffer_load_dwordx4 %1, %3, %4, 0 offen sc1\n\ts_waitcnt vmcnt(0)"
               : "=&v"(a), "=&v"(b) : "v"(oa), "v"(ob), "s"(rs) : "memory");
}
__device__ __forceinline__ void reload3(v4u& a, v4u& b, v4u& c, rsrc_t rs, int oa, int ob, int oc) {
  asm volatile("buffer_load_dwordx4 %0, %3, %6, 0 offen sc1\n\tbuffer_load_dwordx4 %1, %4, %6, 0 offen sc1\n\t"
               "buffer_load_dwordx4 %2, %5, %6, 0 offen sc1\n\ts_waitcnt vmcnt(0)"
               : "=&v"(a), "=&v"(b), "=&v"(c) : "v"(oa), "v"(ob), "v"(oc), "s"(rs) : "memory");
}
__device__ __forceinline__ float gelu_tanh(float x) {
  const float u = 0.7978845608028654f * (x + 0.044715f * x * x * x);
  const float e = __builtin_amdgcn_exp2f(u * 2.885390081777927f);
  const float th = 1.f - 2.f / (e + 1.f);
  return 0.5f * x * (1.f + th);
}
__device__ __forceinline__ float colsum3(const v4u& a, const v4u& b, const v4u& c) {
  float t0 = (asf(a.x) + asf(b.x)) + asf(c.x), t1 = (asf(a.z) + asf(b.z)) + asf(c.z);
  t0 += dpp<ROR4>(t0); t1 += dpp<ROR4>(t1);
  t0 += dpp<ROR8>(t0); t1 += dpp<ROR8>(t1);
  return rb(sum_rows_xy(t0, t1));
}

constexpr int L_RED = 0;
constexpr int L_RED2 = 16 * 192;
constexpr int L_Q = L_RED2 + 16 * 128;
constexpr int L_GU = L_Q + 96;
constexpr int L_WM = L_GU + 64;
constexpr int L_DT = L_WM + 16;
constexpr int L_END = 21504;

__global__ void __launch_bounds__(512, 2)
fused_decode(const u16* x_, const u16* kc_, const u16* vc_, const u16* wq_, const u16* wo_, const u16* wg_, const u16* wu_,
             const u16* wd_, u16* out_, unsigned char* ws_) {
  __shared__ __attribute__((aligned(16))) float lds[L_END];
  const int b = blockIdx.x, xg = b & 7, idx = b >> 3, h = 4 * xg + (idx & 3), s = idx >> 2;
  const int tid = threadIdx.x, lane = tid & 63, w = __builtin_amdgcn_readfirstlane(tid >> 6);
  const int u = lane >> 3, c8 = lane & 7, g = lane >> 4, n16 = lane & 15;
  v16u ka; v4u kb; u64 ts64;
  asm volatile("s_load_dwordx16 %0, %3, 0x0\n\ts_load_dwordx4 %1, %3, 0x40\n\ts_memrealtime %2"
               : "=&s"(ka), "=&s"(kb), "=&s"(ts64) : "s"(__builtin_amdgcn_kernarg_segment_ptr()));
  PIN();
  unsigned cpf0 = 0, cpf1 = 0;
  if (w == 0 && idx >= 30) {
    u64 pc;
    asm volatile("s_getpc_b64 %0" : "=s"(pc));
    const rsrc_t rc = mk((const void*)pc, CODEPF);
    asm volatile("buffer_load_dword %0, %2, %3, 0 offen\n\tbuffer_load_dword %1, %2, %3, %4 offen"
                 : "+v"(cpf0), "+v"(cpf1) : "v"(lane * 128), "s"(rc), "s"(8192));
  }
  PIN();
  const int rgl = idx / 5, cb = idx - 5 * rgl;
  const bool tile = idx < 30;
  int astride = rgl < 4 ? NH * DH * 2 : FF * 2;
  int aoff = 128 * cb * astride + (rgl < 4 ? 1536 * xg + 384 * rgl : 384 * xg);
  int abytes = tile ? 127 * astride + 384 : 0;
  int asel = rgl < 4 ? 0 : rgl - 3;
  int xro = (128 * cb + 16 * w + 2 * u) * 2;
  int ao = (16 * w + 2 * u) * astride + c8 * 16;
  unsigned kvoff = ((unsigned)h * SK + (SK - WIN)) * (DH * 2);
  int kvo = u * 384 + c8 * 16, kvs = w * 3072 + s * 24576;
  asm volatile("" : "+s"(astride), "+s"(aoff), "+s"(abytes), "+s"(asel), "+s"(kvoff), "+s"(kvs), "+v"(xro), "+v"(ao), "+v"(kvo));
  PIN();
  asm volatile("s_waitcnt lgkmcnt(0)" : "+s"(ka), "+s"(kb), "+s"(ts64));
  const unsigned ts = (unsigned)ts64 + EARLY;
#define SLOT0(n) not_before(ts + (((n) * TAU) >> 4))
#define KARG(lo, hi) (((u64)(hi) << 32) | (lo))
  const u16 *x = (const u16*)KARG(ka[0], ka[1]), *kc = (const u16*)KARG(ka[2], ka[3]), *vc = (const u16*)KARG(ka[4], ka[5]);
  const u16 *wq = (const u16*)KARG(ka[6], ka[7]), *wo = (const u16*)KARG(ka[8], ka[9]), *wg = (const u16*)KARG(ka[10], ka[11]);
  const u16 *wu = (const u16*)KARG(ka[12], ka[13]), *wd = (const u16*)KARG(ka[14], ka[15]);
  u16* out = (u16*)KARG(kb[0], kb[1]);
  unsigned char* ws = (unsigned char*)KARG(kb[2], kb[3]);

  const rsrc_t ra = mk((const char*)(asel == 0 ? wq : (asel == 1 ? wg : wu)) + aoff, abytes);
  v4u a[6];
#pragma unroll
  for (int t = 0; t < 3; ++t) a[t] = ld16<AUX_NT>(ra, ao + t * 128);
  PIN();
  const rsrc_t rx = mk(x, D * 2);
  const v4u xs = ld16<0>(rx, lane * 16);
  const unsigned xs4 = __builtin_amdgcn_raw_buffer_load_b32(rx, lane * 4 + 1024, 0, 0);
  const unsigned xr = __builtin_amdgcn_raw_buffer_load_b32(rx, xro, 0, 0);
  PIN();
#pragma unroll
  for (int t = 0; t < 3; ++t) a[3 + t] = ld16<AUX_NT>(ra, ao + t * 128, astride);
  PIN();
  const rsrc_t rws = mk(ws, WS_END);
  const unsigned epoch = __builtin_amdgcn_raw_buffer_load_b32(rws, WS_EPOCH, 0, AUX_SC1);
  const rsrc_t rk = mk((const char*)kc + kvoff, WIN * DH * 2), rv = mk((const char*)vc + kvoff, WIN * DH * 2);
  lockstep();
  v4u kv[48];
#define ISSUE(n) do { _Pragma("unroll") for (int t_ = 0; t_ < 3; ++t_) { \
    kv[3 * (n) + t_] = ld16<AUX_NT>((n) < 8 ? rk : rv, kvo + t_ * 128, kvs + (((n) + h) & 7) * 196608); PIN(); } } while (0)
  ISSUE(0);
  v4u mask;
  {
    const unsigned hm = (lane & 1) ? 0xffff0000u : 0x0000ffffu;
    const int r = c8 >> 1;
    mask = v4u{r == 0 ? hm : 0u, r == 1 ? hm : 0u, r == 2 ? hm : 0u, r == 3 ? hm : 0u};
  }
  pace<10>();
  ISSUE(1);
  float xn0, xn1;
  {
    const unsigned xv[5] = {xs.x, xs.y, xs.z, xs.w, xs4};
    float sum = 0.f;
#pragma unroll
    for (int j = 0; j < 5; ++j) sum += bflo(xv[j]) + bfhi(xv[j]);
    const float mean = wave_sum(sum) * (1.f / D);
    float sq = 0.f;
#pragma unroll
    for (int j = 0; j < 5; ++j) { const float d0 = bflo(xv[j]) - mean, d1 = bfhi(xv[j]) - mean; sq += d0 * d0 + d1 * d1; }
    const float var = rb(wave_sum(sq) * (1.f / D));
    const float mb = rb(mean), sd = rb(sqrtf(rb(var + 1e-5f)));
    xn0 = rb(bflo(xr) - mb) / sd;
    xn1 = rb(bfhi(xr) - mb) / sd;
  }
  asm volatile("" : "+v"(xn0) : "v"(cpf0), "v"(cpf1));
  const bool valid = ((lane >> 3) & 1) == (lane >> 5);
  float* const redp = lds + L_RED + (w * 2 + (lane >> 5)) * 192 + 8 * c8 + 4 * (g & 1);
  {
    const v4u d0 = diag(xn0, mask), d1 = diag(xn1, mask);
    f32x4 acc[3];
#pragma unroll
    for (int t = 0; t < 3; ++t) acc[t] = mfma(d0, a[t], f32x4{0.f, 0.f, 0.f, 0.f});
#pragma unroll
    for (int t = 0; t < 3; ++t) acc[t] = mfma(d1, a[3 + t], acc[t]);
    if (valid) {
#pragma unroll
      for (int t = 0; t < 3; ++t) *(f32x4*)(redp + 64 * t) = acc[t];
    }
    PIN();
  }
  ISSUE(2);
  const unsigned tag = (unsigned)__builtin_amdgcn_readfirstlane((int)epoch) * 0x9E3779B1u + 0x7F4A7C15u;
  wg_barrier();
  const float* const sump = lds + L_RED + 4 * g * 192 + 32 * w + 2 * n16;
#define SUM192() sum_rows2((*(const f32x2*)sump + *(const f32x2*)(sump + 192)) + (*(const f32x2*)(sump + 384) + *(const f32x2*)(sump + 576)))
  unsigned tref;
  {
    f32x2 sum = SUM192();
    tref = clock_lo();
    if (w == 6) sum.x = asf(tref);
    publish(rws, !tile ? OOB : ((w < 6 && lane < 16) ? WS_QP + (xg * 30 + idx) * P_STRIDE + (16 * w + lane) * 16
                                                     : ((tid == 384 && cb == 2) ? WS_T0 + (rgl * 8 + xg) * 16 : OOB)), sum, tag);
  }
  PIN();
  const int yb = (cb * 48 + 6 * xg + rgl) * Y_STRIDE;
  const int ob = WS_OP + (h * 8 + s) * P_STRIDE;
  const rsrc_t rd = mk(wd + (size_t)(32 * (6 * xg + rgl)) * D + 128 * cb, tile ? 31 * D * 2 + 256 : 0);
  const rsrc_t ro = mk(wo + (size_t)(128 * (6 * xg + rgl)) * D + 128 * cb, tile ? 127 * D * 2 + 256 : 0);
  const bool glive = w < 6 || tile;
  const int gb = WS_QP + (xg * 30 + 5 * (w < 6 ? (idx & 3) : w - 2)) * P_STRIDE + (16 * (w < 6 ? w : rgl) + n16) * 16;
  const int go0 = glive ? gb + g * P_STRIDE : OOB;
  const int wl = 48 * w + lane - 16;
  const int go1 = g == 0 ? (glive ? gb + 4 * P_STRIDE : OOB)
                         : (wl < 96 ? ob + wl * 16
                                    : (wl < 224 ? (tile ? (wl < 160 ? WS_YP - 96 * 16 : WS_ZP - 160 * 16) + yb + wl * 16 : OOB)
                                                : (w == 7 ? WS_T0 + (lane - 16) * 16 : OOB)));
  pace<7>(); SLOT0(3); ISSUE(3); lockstep();
  pace<7>(); SLOT0(4); ISSUE(4); lockstep();
  pace<7>(); SLOT0(5);
  v4u g0 = ld16<AUX_SC1>(rws, go0), g1 = ld16<AUX_SC1>(rws, go1);
  lockstep();
  ISSUE(5); lockstep();
  pace<8>(); SLOT0(6); ISSUE(6); lockstep();
  pace<6>(); SLOT0(7); ISSUE(7);
#define GATHER_OK() (!glive || (missing(g0, tag) | (missing(g1, tag) & 0xffff)) == 0)
  if (__builtin_expect(!GATHER_OK(), 0)) {
    unsigned spins = 0;
#pragma unroll 1
    do {
      __builtin_amdgcn_s_sleep(2);
      reload2(g0, g1, rws, go0, go1);
    } while (!GATHER_OK() && ++spins < SPIN_CAP);
  }
  float* const red2p = lds + L_RED2 + w * 128 + 8 * n16 + 4 * (g & 1);
  {
    const f32x2 q = sum_rows2(f32x2{asf(g0.x), asf(g0.z)}) + f32x2{asf(g1.x), asf(g1.z)};
    if (lane < 16) {
      if (w < 6) ((unsigned*)(lds + L_Q))[w * 16 + lane] = pk(q.x, q.y);
      else *(f32x2*)(lds + L_GU + (w - 6) * 32 + 2 * lane) = q;
    }
    if (w == 7) {
      const bool have = g != 0 && tagged(g1, tag);
      int e = have ? (int)(tref - g1.x) : 0;
      e = e < -400 ? -400 : (e > 400 ? 400 : e);
      const float cnt = wave_sum(have ? 1.f : 0.f), em = wave_sum((float)e) * __builtin_amdgcn_rcpf(fmaxf(cnt, 1.f));
      if (lane == 0) lds[L_DT] = em;
    }
  }
  wg_barrier();
  float sc[8];
  v4u qk[3];
#pragma unroll
  for (int t = 0; t < 3; ++t) qk[t] = *(const v4u*)(lds + L_Q + 32 * t + 4 * c8);
  tref += LEAD - (unsigned)__builtin_amdgcn_readfirstlane((int)lds[L_DT]);
  float wmax = -INFINITY;
#define QK(i) do { float d_[3]; \
    _Pragma("unroll") for (int t_ = 0; t_ < 3; ++t_) { d_[t_] = 0.f; _Pragma("unroll") for (int c_ = 0; c_ < 4; ++c_) d_[t_] = dot2(kv[3 * (i) + t_][c_], qk[t_][c_], d_[t_]); } \
    sc[i] = rb(rb(sum8((d_[0] + d_[1]) + d_[2])) * 0.07216878364870322f); wmax = fmaxf(wmax, sc[i]); PIN(); } while (0)
#define SLOT(n) not_before(tref + (((n) * TAU) >> 4))
  QK(0);
  pace<6>(); SLOT(8); ISSUE(8);
  QK(1); QK(2);
  unsigned hp;
  {
    const float ga = lds[L_GU + 4 * w + g], up = lds[L_GU + 32 + 4 * w + g];
    const float hv = rb(gelu_tanh(rb(ga))) * rb(up);
    hp = pk(hv, hv);
    asm volatile("" : "+v"(hp));
  }
  lockstep();
  pace<6>(); SLOT(9); ISSUE(9);
  QK(3); QK(4);
  lockstep();
  pace<6>(); SLOT(10); ISSUE(10);
  QK(5); QK(6); QK(7);
  constexpr float LOG2E = 1.4426950408889634f;
  {
    wmax = max_u(wmax);
    float lsum = 0.f;
#pragma unroll
    for (int i = 0; i < 8; ++i) lsum += __builtin_amdgcn_exp2f((sc[i] - wmax) * LOG2E);
    lsum = sum_u(lsum);
    if (lane == 0) *(f32x2*)(lds + L_WM + 2 * w) = f32x2{wmax, lsum};
  }
  wg_barrier();
  pace<6>(); SLOT(11); ISSUE(11);
  {
    const f32x2 wm = *(const f32x2*)(lds + L_WM + 2 * c8);
    const float M = max8(wm.x);
    publish(rws, tid == 384 ? WS_ML + (h * 8 + s) * 16 : OOB, f32x2{M, sum8(__builtin_amdgcn_exp2f((wm.x - M) * LOG2E) * wm.y)}, tag);
    PIN();
  }
  lockstep();
  pace<7>(); SLOT(12); ISSUE(12); lockstep();
  pace<7>(); SLOT(13); ISSUE(13); lockstep();
  pace<7>(); SLOT(14);
  const int mo = WS_ML + (h * 8 + c8) * 16;
  v4u ml = ld16<AUX_SC1>(rws, mo);
  PIN();
  ISSUE(14); lockstep();
  pace<7>(); SLOT(15); ISSUE(15);
  const int gd = 128 * rgl + 16 * w, hl = (gd * 683) >> 17;
  int o0 = WS_OP + ((4 * xg + hl) * 8 + u) * P_STRIDE + ((gd - 192 * hl) / 2 + c8) * 16;
  int ost = lane < 16 ? ob + (16 * w + lane) * 16 : OOB, yst = WS_YP + yb + lane * 16;
  asm volatile("" : "+v"(o0), "+v"(ost), "+v"(yst));
  lockstep();
  if (__builtin_expect(missing(ml, tag) != 0, 0)) {
    unsigned spins = 0;
#pragma unroll 1
    do { reload1(ml, rws, mo); } while (missing(ml, tag) != 0 && ++spins < SPIN_CAP);
  }
  {
    const float ms = asf(ml.x), M = max8(ms);
    const float inv = 1.f / sum8(__builtin_amdgcn_exp2f((ms - M) * LOG2E) * asf(ml.z));
#pragma unroll
    for (int i = 0; i < 8; ++i) sc[i] = __builtin_amdgcn_exp2f((sc[i] - M) * LOG2E) * inv;
  }
  f32x4 acc[3] = {f32x4{0.f, 0.f, 0.f, 0.f}, f32x4{0.f, 0.f, 0.f, 0.f}, f32x4{0.f, 0.f, 0.f, 0.f}};
#define PV(i) do { const v4u dp_ = diag(sc[i], mask); \
    _Pragma("unroll") for (int t_ = 0; t_ < 3; ++t_) acc[t_] = mfma(dp_, kv[24 + 3 * (i) + t_], acc[t_]); PIN(); } while (0)
#pragma unroll
  for (int i = 0; i < 4; ++i) PV(i);
  SLOT(16);
  const v4u wdr = ld16<AUX_NT>(rd, (4 * w + g) * 1280 + n16 * 16);
  PIN();
  v4u wor[4];
#pragma unroll
  for (int r = 0; r < 4; ++r) wor[r] = ld16<AUX_NT>(ro, (16 * w + 8 * (g >> 1) + (g & 1)) * 1280 + n16 * 16, r * 2560);
  PIN();
#pragma unroll
  for (int i = 4; i < 8; ++i) PV(i);
  if (valid) {
#pragma unroll
    for (int t = 0; t < 3; ++t) *(f32x4*)(redp + 64 * t) = acc[t];
  }
  wg_barrier();
  if (w < 6) publish(rws, ost, SUM192(), tag);
  const float* const sum2p = lds + L_RED2 + (lane >> 5) * 512 + 4 * (lane & 31);
#define SUM128(o) ({ const f32x4 s_ = (*(const f32x4*)(sum2p + (o)) + *(const f32x4*)(sum2p + (o) + 128)) + (*(const f32x4*)(sum2p + (o) + 256) + *(const f32x4*)(sum2p + (o) + 384)); \
    const auto a_ = __builtin_amdgcn_permlane32_swap(asu(s_[0]), asu(s_[2]), false, false), b_ = __builtin_amdgcn_permlane32_swap(asu(s_[1]), asu(s_[3]), false, false); \
    f32x2{asf(a_[0]), asf(b_[0])} + f32x2{asf(a_[1]), asf(b_[1])}; })
  unsigned spins = 0;
  if (tile) {
    const f32x4 z = mfma(v4u{hp, hp, hp, hp} & mask, wdr, f32x4{0.f, 0.f, 0.f, 0.f});
    if (lane < 32) *(f32x4*)(red2p + 1024) = z;
    wg_barrier();
    if (w == 7) publish(rws, yst + (WS_ZP - WS_YP), SUM128(1024), tag);
    if (w != 6) pace<1>(); else pace<0>();
    v4u e0;
#pragma unroll 1
    for (;;) {
      reload1(e0, rws, o0);
      if (missing(e0, tag) == 0) break;
      if (++spins >= SPIN_CAP) break;
    }
    float t0 = asf(e0.x), t1 = asf(e0.z);
    t0 += dpp<ROR8>(t0); t1 += dpp<ROR8>(t1);
    const float ov = sum_rows_xy(t0, t1);
    unsigned op = pk(ov, ov);
    op = (unsigned)__builtin_amdgcn_update_dpp((int)op, (int)op, ROR4, 0xc, 0xf, false);
    f32x4 y = mfma(diag_lane<0>(op, mask), wor[0], f32x4{0.f, 0.f, 0.f, 0.f});
    y = mfma(diag_lane<1>(op, mask), wor[1], y);
    y = mfma(diag_lane<2>(op, mask), wor[2], y);
    y = mfma(diag_lane<3>(op, mask), wor[3], y);
    if (lane < 32) *(f32x4*)red2p = y;
    wg_barrier();
    if (w == 0) publish(rws, yst, SUM128(0), tag);
  } else if (w >= 3) {
    const int unit = 5 * (xg + 8 * (idx - 30)) + w - 3;
    const int slot = 4 * (unit & 15) + (lane & 3);
    const int col = 128 * (unit >> 4) + 4 * (slot & 31) + 2 * (slot >> 5) + (g & 1);
    const int fo = ((unit >> 4) * 48 + (lane >> 2)) * Y_STRIDE + slot * 16;
    float xo = bflo(__builtin_amdgcn_raw_buffer_load_b16(rx, col * 2, 0, 0));
    v4u f0, f1, f2;
#pragma unroll 1
    do {
      reload3(f0, f1, f2, rws, WS_ZP + fo, WS_ZP + fo + 16 * Y_STRIDE, WS_ZP + fo + 32 * Y_STRIDE);
    } while ((missing(f0, tag) | missing(f1, tag) | missing(f2, tag)) != 0 && ++spins < SPIN_CAP);
    const float zz = colsum3(f0, f1, f2);
    float gt = rb(zz / (1.f + __builtin_amdgcn_exp2f(-zz * LOG2E)));
    int oc = (lane < 32 && n16 < 4) ? col * 2 : OOB;
    int y0 = WS_YP + fo, y1 = y0 + 16 * Y_STRIDE, y2 = y0 + 32 * Y_STRIDE;
    asm volatile("" : "+v"(gt), "+v"(xo), "+v"(oc), "+v"(y0), "+v"(y1), "+v"(y2));
    const rsrc_t rout = mk(out, D * 2);
#pragma unroll 1
    for (;;) {
      reload3(f0, f1, f2, rws, y0, y1, y2);
      if ((missing(f0, tag) | missing(f1, tag) | missing(f2, tag)) == 0) break;
      if (++spins >= SPIN_CAP) break;
    }
    __builtin_amdgcn_raw_buffer_store_b16((u16)pk(xo + rb(colsum3(f0, f1, f2) * gt), 0.f), rout, oc, 0, 0);
    if (unit == 0) __builtin_amdgcn_raw_buffer_store_b32(tag, rws, lane == 0 ? WS_EPOCH : OOB, 0, AUX_SC1);
  }
}

extern "C" void kernel_launch(void* const* d_in, const int* in_sizes, int n_in, void* d_out, int out_size, void* d_ws, size_t ws_size,
                              hipStream_t stream) {
  fused_decode<<<256, 512, 0, stream>>>((const u16*)d_in[0], (const u16*)d_in[1], (const u16*)d_in[2], (const u16*)d_in[3],
                                        (const u16*)d_in[4], (const u16*)d_in[6], (const u16*)d_in[7], (const u16*)d_in[8],
                                        (u16*)d_out, (unsigned char*)d_ws);
}
```

```cpp
#include <hip/hip_runtime.h>
#include <hip/hip_bf16.h>

typedef unsigned short u16;
typedef unsigned v4u __attribute__((ext_vector_type(4)));
typedef unsigned v16u __attribute__((ext_vector_type(16)));
typedef float f32x4 __attribute__((ext_vector_type(4)));
typedef float f32x2 __attribute__((ext_vector_type(2)));
typedef __bf16 bf16x8 __attribute__((ext_vector_type(8)));
typedef __bf16 bf16x2 __attribute__((ext_vector_type(2)));
typedef __amdgpu_buffer_rsrc_t rsrc_t;
typedef unsigned long long u64;

constexpr int D = 640, NH = 32, DH = 192, SK = 32768, WIN = 4096, FF = 1536;
constexpr int AUX_NT = 2, AUX_SC1 = 16;
constexpr int OOB = 0x40000000;
constexpr unsigned SPIN_CAP = 1u << 16;

constexpr int WS_EPOCH = 0;
constexpr int P_STRIDE = 1664, Y_STRIDE = 1152;
constexpr int WS_T0 = 2048;
constexpr int WS_ML = 4096;
constexpr int WS_QP = 8192;
constexpr int WS_OP = WS_QP + 240 * P_STRIDE;
constexpr int WS_ZP = WS_OP + 256 * P_STRIDE;
constexpr int WS_YP = WS_ZP + 240 * Y_STRIDE;
constexpr int WS_END = WS_YP + 240 * Y_STRIDE;

#define TAU 1376
#define EARLY 150
#define LEAD -158
#define CODEPF 9300

__device__ __forceinline__ rsrc_t mk(const void* p, int bytes) { return __builtin_amdgcn_make_buffer_rsrc((void*)p, 0, bytes, 0x00020000); }
template <int AUX> __device__ __forceinline__ v4u ld16(rsrc_t r, int voff, int soff = 0) { return __builtin_amdgcn_raw_buffer_load_b128(r, voff, soff, AUX); }
__device__ __forceinline__ float asf(unsigned u) { return __builtin_bit_cast(float, u); }
__device__ __forceinline__ unsigned asu(float f) { return __builtin_bit_cast(unsigned, f); }
__device__ __forceinline__ void publish(rsrc_t r, int voff, f32x2 v, unsigned tag) {
  __builtin_amdgcn_raw_buffer_store_b128(v4u{asu(v.x), tag, asu(v.y), tag}, r, voff, 0, AUX_SC1);
}
__device__ __forceinline__ float bflo(unsigned u) { return asf(u << 16); }
__device__ __forceinline__ float bfhi(unsigned u) { return asf(u & 0xffff0000u); }
__device__ __forceinline__ unsigned pk(float lo, float hi) {
  bf16x2 r = {(__bf16)lo, (__bf16)hi};
  return __builtin_bit_cast(unsigned, r);
}
__device__ __forceinline__ float rb(float f) { return (float)(__bf16)f; }
__device__ __forceinline__ float dot2(unsigned a, unsigned b, float c) {
  return __builtin_amdgcn_fdot2_f32_bf16(__builtin_bit_cast(bf16x2, a), __builtin_bit_cast(bf16x2, b), c, false);
}
template <int CTRL> __device__ __forceinline__ float dpp(float x) {
  return asf((unsigned)__builtin_amdgcn_mov_dpp((int)asu(x), CTRL, 0xf, 0xf, true));
}
constexpr int XOR1 = 0xB1, XOR2 = 0x4E, HMIR = 0x141, ROR8 = 0x128, ROR4 = 0x124, BCAST = 0x150;
__device__ __forceinline__ float sum8(float x) { x += dpp<XOR1>(x); x += dpp<XOR2>(x); x += dpp<HMIR>(x); return x; }
__device__ __forceinline__ float max8(float x) { x = fmaxf(x, dpp<XOR1>(x)); x = fmaxf(x, dpp<XOR2>(x)); x = fmaxf(x, dpp<HMIR>(x)); return x; }
__device__ __forceinline__ float sum_rows(float x) {
  auto s = __builtin_amdgcn_permlane16_swap(asu(x), asu(x), false, false);
  x = asf(s[0]) + asf(s[1]);
  auto t = __builtin_amdgcn_permlane32_swap(asu(x), asu(x), false, false);
  return asf(t[0]) + asf(t[1]);
}
__device__ __forceinline__ float max_rows(float x) {
  auto s = __builtin_amdgcn_permlane16_swap(asu(x), asu(x), false, false);
  x = fmaxf(asf(s[0]), asf(s[1]));
  auto t = __builtin_amdgcn_permlane32_swap(asu(x), asu(x), false, false);
  return fmaxf(asf(t[0]), asf(t[1]));
}
__device__ __forceinline__ float sum_u(float x) { x += dpp<ROR8>(x); return sum_rows(x); }
__device__ __forceinline__ float max_u(float x) { x = fmaxf(x, dpp<ROR8>(x)); return max_rows(x); }
__device__ __forceinline__ float wave_sum(float x) { return sum_u(sum8(x)); }
__device__ __forceinline__ float sum_rows_xy(float x, float y) {
  auto s = __builtin_amdgcn_permlane16_swap(asu(x), asu(y), false, false);
  const float t = asf(s[0]) + asf(s[1]);
  auto r = __builtin_amdgcn_permlane32_swap(asu(t), asu(t), false, false);
  return asf(r[0]) + asf(r[1]);
}
__device__ __forceinline__ f32x2 sum_rows2(f32x2 v) {
  auto a = __builtin_amdgcn_permlane16_swap(asu(v.x), asu(v.x), false, false), b = __builtin_amdgcn_permlane16_swap(asu(v.y), asu(v.y), false, false);
  v = f32x2{asf(a[0]), asf(b[0])} + f32x2{asf(a[1]), asf(b[1])};
  auto c = __builtin_amdgcn_permlane32_swap(asu(v.x), asu(v.x), false, false), d = __builtin_amdgcn_permlane32_swap(asu(v.y), asu(v.y), false, false);
  return f32x2{asf(c[0]), asf(d[0])} + f32x2{asf(c[1]), asf(d[1])};
}
__device__ __forceinline__ v4u diag(float val, const v4u& mask) { const unsigned p = pk(val, val); return v4u{p, p, p, p} & mask; }
template <int L> __device__ __forceinline__ v4u diag_lane(unsigned p, const v4u& mask) {
  const unsigned d = (unsigned)__builtin_amdgcn_mov_dpp((int)p, BCAST + L, 0xf, 0xf, true);
  return v4u{d, d, d, d} & mask;
}
__device__ __forceinline__ f32x4 mfma(v4u a, v4u b, f32x4 c) {
  return __builtin_amdgcn_mfma_f32_16x16x32_bf16(__builtin_bit_cast(bf16x8, a), __builtin_bit_cast(bf16x8, b), c, 0, 0, 0);
}
#define PIN() __builtin_amdgcn_sched_barrier(0)
__device__ __forceinline__ void wg_barrier() {
  asm volatile("s_waitcnt lgkmcnt(0)" ::: "memory");
  __builtin_amdgcn_s_barrier();
  asm volatile("" ::: "memory");
}
__device__ __forceinline__ void lockstep() { PIN(); __builtin_amdgcn_s_barrier(); PIN(); }
template <int N> __device__ __forceinline__ void pace() { PIN(); asm volatile("s_waitcnt vmcnt(%0)" :: "i"(N)); PIN(); }
__device__ __forceinline__ unsigned clock_lo() {
  u64 t;
  asm volatile("s_memrealtime %0\n\ts_waitcnt lgkmcnt(0)" : "=s"(t) :: "memory");
  return (unsigned)t;
}
__device__ __forceinline__ void not_before(unsigned t) {
  PIN();
  asm volatile("1:\n\ts_memrealtime vcc\n\ts_waitcnt lgkmcnt(0)\n\ts_sub_u32 vcc_lo, vcc_lo, %0\n\ts_cmp_lt_i32 vcc_lo, 0\n\ts_cbranch_scc1 1b"
               :: "s"(t) : "vcc", "scc", "memory");
  PIN();
}
__device__ __forceinline__ bool tagged(const v4u& g, unsigned tag) { return (g.y == tag) & (g.w == tag); }
__device__ __forceinline__ u64 missing(const v4u& g, unsigned tag) { return __builtin_amdgcn_ballot_w64(g.y != tag) | __builtin_amdgcn_ballot_w64(g.w != tag); }
__device__ __forceinline__ void reload1(v4u& a, rsrc_t rs, int oa) {
  asm volatile("buffer_load_dwordx4 %0, %1, %2, 0 offen sc1\n\ts_waitcnt vmcnt(0)" : "=&v"(a) : "v"(oa), "s"(rs) : "memory");
}
__device__ __forceinline__ void reload2(v4u& a, v4u& b, rsrc_t rs, int oa, int ob) {
  asm volatile("buffer_load_dwordx4 %0, %2, %4, 0 offen sc1\n\tbuffer_load_dwordx4 %1, %3, %4, 0 offen sc1\n\ts_waitcnt vmcnt(0)"
               : "=&v"(a), "=&v"(b) : "v"(oa), "v"(ob), "s"(rs) : "memory");
}
__device__ __forceinline__ void reload3(v4u& a, v4u& b, v4u& c, rsrc_t rs, int oa, int ob, int oc) {
  asm volatile("buffer_load_dwordx4 %0, %3, %6, 0 offen sc1\n\tbuffer_load_dwordx4 %1, %4, %6, 0 offen sc1\n\t"
               "buffer_load_dwordx4 %2, %5, %6, 0 offen sc1\n\ts_waitcnt vmcnt(0)"
               : "=&v"(a), "=&v"(b), "=&v"(c) : "v"(oa), "v"(ob), "v"(oc), "s"(rs) : "memory");
}
__device__ __forceinline__ float gelu_tanh(float x) {
  const float u = 0.7978845608028654f * (x + 0.044715f * x * x * x);
  const float e = __builtin_amdgcn_exp2f(u * 2.885390081777927f);
  const float th = 1.f - 2.f / (e + 1.f);
  return 0.5f * x * (1.f + th);
}
__device__ __forceinline__ float colsum3(const v4u& a, const v4u& b, const v4u& c) {
  float t0 = (asf(a.x) + asf(b.x)) + asf(c.x), t1 = (asf(a.z) + asf(b.z)) + asf(c.z);
  t0 += dpp<ROR4>(t0); t1 += dpp<ROR4>(t1);
  t0 += dpp<ROR8>(t0); t1 += dpp<ROR8>(t1);
  return rb(sum_rows_xy(t0, t1));
}

constexpr int L_RED = 0;
constexpr int L_RED2 = 16 * 192;
constexpr int L_Q = L_RED2 + 16 * 128;
constexpr int L_GU = L_Q + 96;
constexpr int L_WM = L_GU + 64;
constexpr int L_DT = L_WM + 16;
constexpr int L_END = 21504;

__global__ void __launch_bounds__(512, 2)
fused_decode(const u16* x_, const u16* kc_, const u16* vc_, const u16* wq_, const u16* wo_, const u16* wg_, const u16* wu_,
             const u16* wd_, u16* out_, unsigned char* ws_) {
  __shared__ __attribute__((aligned(16))) float lds[L_END];
  const int b = blockIdx.x, xg = b & 7, idx = b >> 3, h = 4 * xg + (idx & 3), s = idx >> 2;
  const int tid = threadIdx.x, lane = tid & 63, w = __builtin_amdgcn_readfirstlane(tid >> 6);
  const int u = lane >> 3, c8 = lane & 7, g = lane >> 4, n16 = lane & 15;
  v16u ka; v4u kb; u64 ts64;
  asm volatile("s_load_dwordx16 %0, %3, 0x0\n\ts_load_dwordx4 %1, %3, 0x40\n\ts_memrealtime %2"
               : "=&s"(ka), "=&s"(kb), "=&s"(ts64) : "s"(__builtin_amdgcn_kernarg_segment_ptr()));
  PIN();
  unsigned cpf0 = 0, cpf1 = 0;
  if (w == 0 && idx >= 30) {
    u64 pc;
    asm volatile("s_getpc_b64 %0" : "=s"(pc));
    const rsrc_t rc = mk((const void*)pc, CODEPF);
    asm volatile("buffer_load_dword %0, %2, %3, 0 offen\n\tbuffer_load_dword %1, %2, %3, %4 offen"
                 : "+v"(cpf0), "+v"(cpf1) : "v"(lane * 128), "s"(rc), "s"(8192));
  }
  PIN();
  const int rgl = idx / 5, cb = idx - 5 * rgl;
  const bool tile = idx < 30;
  int astride = rgl < 4 ? NH * DH * 2 : FF * 2;
  int aoff = 128 * cb * astride + (rgl < 4 ? 1536 * xg + 384 * rgl : 384 * xg);
  int abytes = tile ? 127 * astride + 384 : 0;
  int asel = rgl < 4 ? 0 : rgl - 3;
  int xro = (128 * cb + 16 * w + 2 * u) * 2;
  int ao = (16 * w + 2 * u) * astride + c8 * 16;
  unsigned kvoff = ((unsigned)h * SK + (SK - WIN)) * (DH * 2);
  int kvo = u * 384 + c8 * 16, kvs = w * 3072 + s * 24576;
  asm volatile("" : "+s"(astride), "+s"(aoff), "+s"(abytes), "+s"(asel), "+s"(kvoff), "+s"(kvs), "+v"(xro), "+v"(ao), "+v"(kvo));
  PIN();
  asm volatile("s_waitcnt lgkmcnt(0)" : "+s"(ka), "+s"(kb), "+s"(ts64));
  const unsigned ts = (unsigned)ts64 + EARLY;
#define SLOT0(n) not_before(ts + (((n) * TAU) >> 4))
#define KARG(lo, hi) (((u64)(hi) << 32) | (lo))
  const u16 *x = (const u16*)KARG(ka[0], ka[1]), *kc = (const u16*)KARG(ka[2], ka[3]), *vc = (const u16*)KARG(ka[4], ka[5]);
  const u16 *wq = (const u16*)KARG(ka[6], ka[7]), *wo = (const u16*)KARG(ka[8], ka[9]), *wg = (const u16*)KARG(ka[10], ka[11]);
  const u16 *wu = (const u16*)KARG(ka[12], ka[13]), *wd = (const u16*)KARG(ka[14], ka[15]);
  u16* out = (u16*)KARG(kb[0], kb[1]);
  unsigned char* ws = (unsigned char*)KARG(kb[2], kb[3]);

  const rsrc_t ra = mk((const char*)(asel == 0 ? wq : (asel == 1 ? wg : wu)) + aoff, abytes);
  v4u a[6];
#pragma unroll
  for (int t = 0; t < 3; ++t) a[t] = ld16<AUX_NT>(ra, ao + t * 128);
  PIN();
  const rsrc_t rx = mk(x, D * 2);
  const v4u xs = ld16<0>(rx, lane * 16);
  const unsigned xs4 = __builtin_amdgcn_raw_buffer_load_b32(rx, lane * 4 + 1024, 0, 0);
  const unsigned xr = __builtin_amdgcn_raw_buffer_load_b32(rx, xro, 0, 0);
  PIN();
#pragma unroll
  for (int t = 0; t < 3; ++t) a[3 + t] = ld16<AUX_NT>(ra, ao + t * 128, astride);
  PIN();
  const rsrc_t rws = mk(ws, WS_END);
  const unsigned epoch = __builtin_amdgcn_raw_buffer_load_b32(rws, WS_EPOCH, 0, AUX_SC1);
  const rsrc_t rk = mk((const char*)kc + kvoff, WIN * DH * 2), rv = mk((const char*)vc + kvoff, WIN * DH * 2);
  lockstep();
  v4u kv[48];
#define ISSUE(n) do { _Pragma("unroll") for (int t_ = 0; t_ < 3; ++t_) { \
    kv[3 * (n) + t_] = ld16<AUX_NT>((n) < 8 ? rk : rv, kvo + t_ * 128, kvs + (((n) + h) & 7) * 196608); PIN(); } } while (0)
  ISSUE(0);
  v4u mask;
  {
    const unsigned hm = (lane & 1) ? 0xffff0000u : 0x0000ffffu;
    const int r = c8 >> 1;
    mask = v4u{r == 0 ? hm : 0u, r == 1 ? hm : 0u, r == 2 ? hm : 0u, r == 3 ? hm : 0u};
  }
  pace<10>();
  ISSUE(1);
  float xn0, xn1;
  {
    const unsigned xv[5] = {xs.x, xs.y, xs.z, xs.w, xs4};
    float sum = 0.f;
#pragma unroll
    for (int j = 0; j < 5; ++j) sum += bflo(xv[j]) + bfhi(xv[j]);
    const float mean = wave_sum(sum) * (1.f / D);
    float sq = 0.f;
#pragma unroll
    for (int j = 0; j < 5; ++j) { const float d0 = bflo(xv[j]) - mean, d1 = bfhi(xv[j]) - mean; sq += d0 * d0 + d1 * d1; }
    const float var = rb(wave_sum(sq) * (1.f / D));
    const float mb = rb(mean), sd = rb(sqrtf(rb(var + 1e-5f)));
    xn0 = rb(bflo(xr) - mb) / sd;
    xn1 = rb(bfhi(xr) - mb) / sd;
  }
  asm volatile("" : "+v"(xn0) : "v"(cpf0), "v"(cpf1));
  const bool valid = ((lane >> 3) & 1) == (lane >> 5);
  float* const redp = lds + L_RED + (w * 2 + (lane >> 5)) * 192 + 8 * c8 + 4 * (g & 1);
  {
    const v4u d0 = diag(xn0, mask), d1 = diag(xn1, mask);
    f32x4 acc[3];
#pragma unroll
    for (int t = 0; t < 3; ++t) acc[t] = mfma(d0, a[t], f32x4{0.f, 0.f, 0.f, 0.f});
#pragma unroll
    for (int t = 0; t < 3; ++t) acc[t] = mfma(d1, a[3 + t], acc[t]);
    if (valid) {
#pragma unroll
      for (int t = 0; t < 3; ++t) *(f32x4*)(redp + 64 * t) = acc[t];
    }
    PIN();
  }
  ISSUE(2);
  const unsigned tag = (unsigned)__builtin_amdgcn_readfirstlane((int)epoch) * 0x9E3779B1u + 0x7F4A7C15u;
  wg_barrier();
  const float* const sump = lds + L_RED + 4 * g * 192 + 32 * w + 2 * n16;
#define SUM192() sum_rows2((*(const f32x2*)sump + *(const f32x2*)(sump + 192)) + (*(const f32x2*)(sump + 384) + *(const f32x2*)(sump + 576)))
  unsigned tref;
  {
    f32x2 sum = SUM192();
    tref = clock_lo();
    if (w == 6) sum.x = asf(tref);
    publish(rws, !tile ? OOB : ((w < 6 && lane < 16) ? WS_QP + (xg * 30 + idx) * P_STRIDE + (16 * w + lane) * 16
                                                     : ((tid == 384 && cb == 2) ? WS_T0 + (rgl * 8 + xg) * 16 : OOB)), sum, tag);
  }
  PIN();
  const int yb = (cb * 48 + 6 * xg + rgl) * Y_STRIDE;
  const int ob = WS_OP + (h * 8 + s) * P_STRIDE;
  const rsrc_t rd = mk(wd + (size_t)(32 * (6 * xg + rgl)) * D + 128 * cb, tile ? 31 * D * 2 + 256 : 0);
  const rsrc_t ro = mk(wo + (size_t)(128 * (6 * xg + rgl)) * D + 128 * cb, tile ? 127 * D * 2 + 256 : 0);
  const bool glive = w < 6 || tile;
  const int gb = WS_QP + (xg * 30 + 5 * (w < 6 ? (idx & 3) : w - 2)) * P_STRIDE + (16 * (w < 6 ? w : rgl) + n16) * 16;
  const int go0 = glive ? gb + g * P_STRIDE : OOB;
  const int wl = 48 * w + lane - 16;
  const int go1 = g == 0 ? (glive ? gb + 4 * P_STRIDE : OOB)
                         : (wl < 96 ? ob + wl * 16
                                    : (wl < 224 ? (tile ? (wl < 160 ? WS_YP - 96 * 16 : WS_ZP - 160 * 16) + yb + wl * 16 : OOB)
                                                : (w == 7 ? WS_T0 + (lane - 16) * 16 : OOB)));
  pace<7>(); SLOT0(3); ISSUE(3); lockstep();
  pace<7>(); SLOT0(4); ISSUE(4); lockstep();
  pace<7>(); SLOT0(5);
  v4u g0 = ld16<AUX_SC1>(rws, go0), g1 = ld16<AUX_SC1>(rws, go1);
  lockstep();
  ISSUE(5); lockstep();
  pace<8>(); SLOT0(6); ISSUE(6); lockstep();
  pace<6>(); SLOT0(7); ISSUE(7);
#define GATHER_OK() (!glive || (missing(g0, tag) | (missing(g1, tag) & 0xffff)) == 0)
  if (__builtin_expect(!GATHER_OK(), 0)) {
    unsigned spins = 0;
#pragma unroll 1
    do {
      __builtin_amdgcn_s_sleep(2);
      reload2(g0, g1, rws, go0, go1);
    } while (!GATHER_OK() && ++spins < SPIN_CAP);
  }
  float* const red2p = lds + L_RED2 + w * 128 + 8 * n16 + 4 * (g & 1);
  {
    const f32x2 q = sum_rows2(f32x2{asf(g0.x), asf(g0.z)}) + f32x2{asf(g1.x), asf(g1.z)};
    if (lane < 16) {
      if (w < 6) ((unsigned*)(lds + L_Q))[w * 16 + lane] = pk(q.x, q.y);
      else *(f32x2*)(lds + L_GU + (w - 6) * 32 + 2 * lane) = q;
    }
    if (w == 7) {
      const bool have = g != 0 && tagged(g1, tag);
      int e = have ? (int)(tref - g1.x) : 0;
      e = e < -400 ? -400 : (e > 400 ? 400 : e);
      const float cnt = wave_sum(have ? 1.f : 0.f), em = wave_sum((float)e) * __builtin_amdgcn_rcpf(fmaxf(cnt, 1.f));
      if (lane == 0) lds[L_DT] = em;
    }
  }
  wg_barrier();
  float sc[8];
  v4u qk[3];
#pragma unroll
  for (int t = 0; t < 3; ++t) qk[t] = *(const v4u*)(lds + L_Q + 32 * t + 4 * c8);
  tref += LEAD - (unsigned)__builtin_amdgcn_readfirstlane((int)lds[L_DT]);
  float wmax = -INFINITY;
#define QK(i) do { float d_[3]; \
    _Pragma("unroll") for (int t_ = 0; t_ < 3; ++t_) { d_[t_] = 0.f; _Pragma("unroll") for (int c_ = 0; c_ < 4; ++c_) d_[t_] = dot2(kv[3 * (i) + t_][c_], qk[t_][c_], d_[t_]); } \
    sc[i] = rb(rb(sum8((d_[0] + d_[1]) + d_[2])) * 0.07216878364870322f); wmax = fmaxf(wmax, sc[i]); PIN(); } while (0)
#define SLOT(n) not_before(tref + (((n) * TAU) >> 4))
  QK(0);
  pace<6>(); SLOT(8); ISSUE(8);
  QK(1); QK(2);
  unsigned hp;
  {
    const float ga = lds[L_GU + 4 * w + g], up = lds[L_GU + 32 + 4 * w + g];
    const float hv = rb(gelu_tanh(rb(ga))) * rb(up);
    hp = pk(hv, hv);
    asm volatile("" : "+v"(hp));
  }
  lockstep();
  pace<6>(); SLOT(9); ISSUE(9);
  QK(3); QK(4);
  lockstep();
  pace<6>(); SLOT(10); ISSUE(10);
  QK(5); QK(6); QK(7);
  constexpr float LOG2E = 1.4426950408889634f;
  {
    wmax = max_u(wmax);
    float lsum = 0.f;
#pragma unroll
    for (int i = 0; i < 8; ++i) lsum += __builtin_amdgcn_exp2f((sc[i] - wmax) * LOG2E);
    lsum = sum_u(lsum);
    if (lane == 0) *(f32x2*)(lds + L_WM + 2 * w) = f32x2{wmax, lsum};
  }
  wg_barrier();
  pace<6>(); SLOT(11); ISSUE(11);
  {
    const f32x2 wm = *(const f32x2*)(lds + L_WM + 2 * c8);
    const float M = max8(wm.x);
    publish(rws, tid == 384 ? WS_ML + (h * 8 + s) * 16 : OOB, f32x2{M, sum8(__builtin_amdgcn_exp2f((wm.x - M) * LOG2E) * wm.y)}, tag);
    PIN();
  }
  lockstep();
  pace<7>(); SLOT(12); ISSUE(12); lockstep();
  pace<7>(); SLOT(13); ISSUE(13); lockstep();
  pace<7>(); SLOT(14);
  const int mo = WS_ML + (h * 8 + c8) * 16;
  v4u ml = ld16<AUX_SC1>(rws, mo);
  PIN();
  ISSUE(14); lockstep();
  pace<7>(); SLOT(15); ISSUE(15);
  const int gd = 128 * rgl + 16 * w, hl = (gd * 683) >> 17;
  int o0 = WS_OP + ((4 * xg + hl) * 8 + u) * P_STRIDE + ((gd - 192 * hl) / 2 + c8) * 16;
  int ost = lane < 16 ? ob + (16 * w + lane) * 16 : OOB, yst = WS_YP + yb + lane * 16;
  asm volatile("" : "+v"(o0), "+v"(ost), "+v"(yst));
  lockstep();
  if (__builtin_expect(missing(ml, tag) != 0, 0)) {
    unsigned spins = 0;
#pragma unroll 1
    do { reload1(ml, rws, mo); } while (missing(ml, tag) != 0 && ++spins < SPIN_CAP);
  }
  {
    const float ms = asf(ml.x), M = max8(ms);
    const float inv = 1.f / sum8(__builtin_amdgcn_exp2f((ms - M) * LOG2E) * asf(ml.z));
#pragma unroll
    for (int i = 0; i < 8; ++i) sc[i] = __builtin_amdgcn_exp2f((sc[i] - M) * LOG2E) * inv;
  }
  f32x4 acc[3] = {f32x4{0.f, 0.f, 0.f, 0.f}, f32x4{0.f, 0.f, 0.f, 0.f}, f32x4{0.f, 0.f, 0.f, 0.f}};
#define PV(i) do { const v4u dp_ = diag(sc[i], mask); \
    _Pragma("unroll") for (int t_ = 0; t_ < 3; ++t_) acc[t_] = mfma(dp_, kv[24 + 3 * (i) + t_], acc[t_]); PIN(); } while (0)
#pragma unroll
  for (int i = 0; i < 4; ++i) PV(i);
  SLOT(16);
  const v4u wdr = ld16<AUX_NT>(rd, (4 * w + g) * 1280 + n16 * 16);
  PIN();
  v4u wor[4];
#pragma unroll
  for (int r = 0; r < 4; ++r) wor[r] = ld16<AUX_NT>(ro, (16 * w + 8 * (g >> 1) + (g & 1)) * 1280 + n16 * 16, r * 2560);
  PIN();
#pragma unroll
  for (int i = 4; i < 8; ++i) PV(i);
  if (valid) {
#pragma unroll
    for (int t = 0; t < 3; ++t) *(f32x4*)(redp + 64 * t) = acc[t];
  }
  wg_barrier();
  if (w < 6) publish(rws, ost, SUM192(), tag);
  const float* const sum2p = lds + L_RED2 + (lane >> 5) * 512 + 4 * (lane & 31);
#define SUM128(o) ({ const f32x4 s_ = (*(const f32x4*)(sum2p + (o)) + *(const f32x4*)(sum2p + (o) + 128)) + (*(const f32x4*)(sum2p + (o) + 256) + *(const f32x4*)(sum2p + (o) + 384)); \
    const auto a_ = __builtin_amdgcn_permlane32_swap(asu(s_[0]), asu(s_[2]), false, false), b_ = __builtin_amdgcn_permlane32_swap(asu(s_[1]), asu(s_[3]), false, false); \
    f32x2{asf(a_[0]), asf(b_[0])} + f32x2{asf(a_[1]), asf(b_[1])}; })
  unsigned spins = 0;
  if (tile) {
    const f32x4 z = mfma(v4u{hp, hp, hp, hp} & mask, wdr, f32x4{0.f, 0.f, 0.f, 0.f});
    if (lane < 32) *(f32x4*)(red2p + 1024) = z;
    wg_barrier();
    if (w == 7) publish(rws, yst + (WS_ZP - WS_YP), SUM128(1024), tag);
    if (w != 6) pace<1>(); else pace<0>();
    v4u e0;
#pragma unroll 1
    for (;;) {
      reload1(e0, rws, o0);
      if (missing(e0, tag) == 0) break;
      if (++spins >= SPIN_CAP) break;
    }
    float t0 = asf(e0.x), t1 = asf(e0.z);
    t0 += dpp<ROR8>(t0); t1 += dpp<ROR8>(t1);
    const float ov = sum_rows_xy(t0, t1);
    unsigned op = pk(ov, ov);
    op = (unsigned)__builtin_amdgcn_update_dpp((int)op, (int)op, ROR4, 0xc, 0xf, false);
    f32x4 y = mfma(diag_lane<0>(op, mask), wor[0], f32x4{0.f, 0.f, 0.f, 0.f});
    y = mfma(diag_lane<1>(op, mask), wor[1], y);
    y = mfma(diag_lane<2>(op, mask), wor[2], y);
    y = mfma(diag_lane<3>(op, mask), wor[3], y);
    if (lane < 32) *(f32x4*)red2p = y;
    wg_barrier();
    if (w == 0) publish(rws, yst, SUM128(0), tag);
  } else if (w >= 3) {
    const int unit = 5 * (xg + 8 * (idx - 30)) + w - 3;
    const int slot = 4 * (unit & 15) + (lane & 3);
    const int col = 128 * (unit >> 4) + 4 * (slot & 31) + 2 * (slot >> 5) + (g & 1);
    const int fo = ((unit >> 4) * 48 + (lane >> 2)) * Y_STRIDE + slot * 16;
    float xo = bflo(__builtin_amdgcn_raw_buffer_load_b16(rx, col * 2, 0, 0));
    v4u f0, f1, f2;
#pragma unroll 1
    do {
      reload3(f0, f1, f2, rws, WS_ZP + fo, WS_ZP + fo + 16 * Y_STRIDE, WS_ZP + fo + 32 * Y_STRIDE);
    } while ((missing(f0, tag) | missing(f1, tag) | missing(f2, tag)) != 0 && ++spins < SPIN_CAP);
    const float zz = colsum3(f0, f1, f2);
    float gt = rb(zz / (1.f + __builtin_amdgcn_exp2f(-zz * LOG2E)));
    int oc = (lane < 32 && n16 < 4) ? col * 2 : OOB;
    int y0 = WS_YP + fo, y1 = y0 + 16 * Y_STRIDE, y2 = y0 + 32 * Y_STRIDE;
    asm volatile("" : "+v"(gt), "+v"(xo), "+v"(oc), "+v"(y0), "+v"(y1), "+v"(y2));
    const rsrc_t rout = mk(out, D * 2);
#pragma unroll 1
    for (;;) {
      reload3(f0, f1, f2, rws, y0, y1, y2);
      if ((missing(f0, tag) | missing(f1, tag) | missing(f2, tag)) == 0) break;
      if (++spins >= SPIN_CAP) break;
    }
    __builtin_amdgcn_raw_buffer_store_b16((u16)pk(xo + rb(colsum3(f0, f1, f2) * gt), 0.f), rout, oc, 0, 17);
    if (unit == 0) __builtin_amdgcn_raw_buffer_store_b32(tag, rws, lane == 0 ? WS_EPOCH : OOB, 0, AUX_SC1);
  }
}

extern "C" void kernel_launch(void* const* d_in, const int* in_sizes, int n_in, void* d_out, int out_size, void* d_ws, size_t ws_size,
                              hipStream_t stream) {
  fused_decode<<<256, 512, 0, stream>>>((const u16*)d_in[0], (const u16*)d_in[1], (const u16*)d_in[2], (const u16*)d_in[3],
                                        (const u16*)d_in[4], (const u16*)d_in[6], (const u16*)d_in[7], (const u16*)d_in[8],
                                        (u16*)d_out, (unsigned char*)d_ws);
}
```

```cpp
#include <hip/hip_runtime.h>
#include <hip/hip_bf16.h>

typedef unsigned short u16;
typedef unsigned v4u __attribute__((ext_vector_type(4)));
typedef unsigned v16u __attribute__((ext_vector_type(16)));
typedef float f32x4 __attribute__((ext_vector_type(4)));
typedef float f32x2 __attribute__((ext_vector_type(2)));
typedef __bf16 bf16x8 __attribute__((ext_vector_type(8)));
typedef __bf16 bf16x2 __attribute__((ext_vector_type(2)));
typedef __amdgpu_buffer_rsrc_t rsrc_t;
typedef unsigned long long u64;

constexpr int D = 640, NH = 32, DH = 192, SK = 32768, WIN = 4096, FF = 1536;
constexpr int AUX_NT = 2, AUX_SC1 = 16;
constexpr int OOB = 0x40000000;
constexpr unsigned SPIN_CAP = 1u << 16;

constexpr int WS_EPOCH = 0;
constexpr int P_STRIDE = 1664, Y_STRIDE = 1152;
constexpr int WS_T0 = 2048;
constexpr int WS_ML = 4096;
constexpr int WS_QP = 8192;
constexpr int WS_OP = WS_QP + 240 * P_STRIDE;
constexpr int WS_ZP = WS_OP + 256 * P_STRIDE;
constexpr int WS_YP = WS_ZP + 240 * Y_STRIDE;
constexpr int WS_END = WS_YP + 240 * Y_STRIDE;

#define TAU 1376
#define EARLY 150
#define LEAD -158
#define CODEPF 9300

__device__ __forceinline__ rsrc_t mk(const void* p, int bytes) { return __builtin_amdgcn_make_buffer_rsrc((void*)p, 0, bytes, 0x00020000); }
template <int AUX> __device__ __forceinline__ v4u ld16(rsrc_t r, int voff, int soff = 0) { return __builtin_amdgcn_raw_buffer_load_b128(r, voff, soff, AUX); }
__device__ __forceinline__ float asf(unsigned u) { return __builtin_bit_cast(float, u); }
__device__ __forceinline__ unsigned asu(float f) { return __builtin_bit_cast(unsigned, f); }
__device__ __forceinline__ void publish(rsrc_t r, int voff, f32x2 v, unsigned tag) {
  __builtin_amdgcn_raw_buffer_store_b128(v4u{asu(v.x), tag, asu(v.y), tag}, r, voff, 0, AUX_SC1);
}
__device__ __forceinline__ float bflo(unsigned u) { return asf(u << 16); }
__device__ __forceinline__ float bfhi(unsigned u) { return asf(u & 0xffff0000u); }
__device__ __forceinline__ unsigned pk(float lo, float hi) {
  bf16x2 r = {(__bf16)lo, (__bf16)hi};
  return __builtin_bit_cast(unsigned, r);
}
__device__ __forceinline__ float rb(float f) { return (float)(__bf16)f; }
__device__ __forceinline__ float dot2(unsigned a, unsigned b, float c) {
  return __builtin_amdgcn_fdot2_f32_bf16(__builtin_bit_cast(bf16x2, a), __builtin_bit_cast(bf16x2, b), c, false);
}
template <int CTRL> __device__ __forceinline__ float dpp(float x) {
  return asf((unsigned)__builtin_amdgcn_mov_dpp((int)asu(x), CTRL, 0xf, 0xf, true));
}
constexpr int XOR1 = 0xB1, XOR2 = 0x4E, HMIR = 0x141, ROR8 = 0x128, ROR4 = 0x124, BCAST = 0x150;
__device__ __forceinline__ float sum8(float x) { x += dpp<XOR1>(x); x += dpp<XOR2>(x); x += dpp<HMIR>(x); return x; }
__device__ __forceinline__ float max8(float x) { x = fmaxf(x, dpp<XOR1>(x)); x = fmaxf(x, dpp<XOR2>(x)); x = fmaxf(x, dpp<HMIR>(x)); return x; }
__device__ __forceinline__ float sum_rows(float x) {
  auto s = __builtin_amdgcn_permlane16_swap(asu(x), asu(x), false, false);
  x = asf(s[0]) + asf(s[1]);
  auto t = __builtin_amdgcn_permlane32_swap(asu(x), asu(x), false, false);
  return asf(t[0]) + asf(t[1]);
}
__device__ __forceinline__ float max_rows(float x) {
  auto s = __builtin_amdgcn_permlane16_swap(asu(x), asu(x), false, false);
  x = fmaxf(asf(s[0]), asf(s[1]));
  auto t = __builtin_amdgcn_permlane32_swap(asu(x), asu(x), false, false);
  return fmaxf(asf(t[0]), asf(t[1]));
}
__device__ __forceinline__ float sum_u(float x) { x += dpp<ROR8>(x); return sum_rows(x); }
__device__ __forceinline__ float max_u(float x) { x = fmaxf(x, dpp<ROR8>(x)); return max_rows(x); }
__device__ __forceinline__ float wave_sum(float x) { return sum_u(sum8(x)); }
__device__ __forceinline__ float sum_rows_xy(float x, float y) {
  auto s = __builtin_amdgcn_permlane16_swap(asu(x), asu(y), false, false);
  const float t = asf(s[0]) + asf(s[1]);
  auto r = __builtin_amdgcn_permlane32_swap(asu(t), asu(t), false, false);
  return asf(r[0]) + asf(r[1]);
}
__device__ __forceinline__ f32x2 sum_rows2(f32x2 v) {
  auto a = __builtin_amdgcn_permlane16_swap(asu(v.x), asu(v.x), false, false), b = __builtin_amdgcn_permlane16_swap(asu(v.y), asu(v.y), false, false);
  v = f32x2{asf(a[0]), asf(b[0])} + f32x2{asf(a[1]), asf(b[1])};
  auto c = __builtin_amdgcn_permlane32_swap(asu(v.x), asu(v.x), false, false), d = __builtin_amdgcn_permlane32_swap(asu(v.y), asu(v.y), false, false);
  return f32x2{asf(c[0]), asf(d[0])} + f32x2{asf(c[1]), asf(d[1])};
}
__device__ __forceinline__ v4u diag(float val, const v4u& mask) { const unsigned p = pk(val, val); return v4u{p, p, p, p} & mask; }
template <int L> __device__ __forceinline__ v4u diag_lane(unsigned p, const v4u& mask) {
  const unsigned d = (unsigned)__builtin_amdgcn_mov_dpp((int)p, BCAST + L, 0xf, 0xf, true);
  return v4u{d, d, d, d} & mask;
}
__device__ __forceinline__ f32x4 mfma(v4u a, v4u b, f32x4 c) {
  return __builtin_amdgcn_mfma_f32_16x16x32_bf16(__builtin_bit_cast(bf16x8, a), __builtin_bit_cast(bf16x8, b), c, 0, 0, 0);
}
#define PIN() __builtin_amdgcn_sched_barrier(0)
__device__ __forceinline__ void wg_barrier() {
  asm volatile("s_waitcnt lgkmcnt(0)" ::: "memory");
  __builtin_amdgcn_s_barrier();
  asm volatile("" ::: "memory");
}
__device__ __forceinline__ void lockstep() { PIN(); __builtin_amdgcn_s_barrier(); PIN(); }
template <int N> __device__ __forceinline__ void pace() { PIN(); asm volatile("s_waitcnt vmcnt(%0)" :: "i"(N)); PIN(); }
__device__ __forceinline__ unsigned clock_lo() {
  u64 t;
  asm volatile("s_memrealtime %0\n\ts_waitcnt lgkmcnt(0)" : "=s"(t) :: "memory");
  return (unsigned)t;
}
__device__ __forceinline__ void not_before(unsigned t) {
  PIN();
  asm volatile("1:\n\ts_memrealtime vcc\n\ts_waitcnt lgkmcnt(0)\n\ts_sub_u32 vcc_lo, vcc_lo, %0\n\ts_cmp_lt_i32 vcc_lo, 0\n\ts_cbranch_scc1 1b"
               :: "s"(t) : "vcc", "scc", "memory");
  PIN();
}
__device__ __forceinline__ bool tagged(const v4u& g, unsigned tag) { return (g.y == tag) & (g.w == tag); }
__device__ __forceinline__ u64 missing(const v4u& g, unsigned tag) { return __builtin_amdgcn_ballot_w64(g.y != tag) | __builtin_amdgcn_ballot_w64(g.w != tag); }
__device__ __forceinline__ void reload1(v4u& a, rsrc_t rs, int oa) {
  asm volatile("buffer_load_dwordx4 %0, %1, %2, 0 offen sc1\n\ts_waitcnt vmcnt(0)" : "=&v"(a) : "v"(oa), "s"(rs) : "memory");
}
__device__ __forceinline__ void reload2(v4u& a, v4u& b, rsrc_t rs, int oa, int ob) {
  asm volatile("buffer_load_dwordx4 %0, %2, %4, 0 offen sc1\n\tbuffer_load_dwordx4 %1, %3, %4, 0 offen sc1\n\ts_waitcnt vmcnt(0)"
               : "=&v"(a), "=&v"(b) : "v"(oa), "v"(ob), "s"(rs) : "memory");
}
__device__ __forceinline__ void reload3(v4u& a, v4u& b, v4u& c, rsrc_t rs, int oa, int ob, int oc) {
  asm volatile("buffer_load_dwordx4 %0, %3, %6, 0 offen sc1\n\tbuffer_load_dwordx4 %1, %4, %6, 0 offen sc1\n\t"
               "buffer_load_dwordx4 %2, %5, %6, 0 offen sc1\n\ts_waitcnt vmcnt(0)"
               : "=&v"(a), "=&v"(b), "=&v"(c) : "v"(oa), "v"(ob), "v"(oc), "s"(rs) : "memory");
}
__device__ __forceinline__ float gelu_tanh(float x) {
  const float u = 0.7978845608028654f * (x + 0.044715f * x * x * x);
  const float e = __builtin_amdgcn_exp2f(u * 2.885390081777927f);
  const float th = 1.f - 2.f / (e + 1.f);
  return 0.5f * x * (1.f + th);
}
__device__ __forceinline__ float colsum3(const v4u& a, const v4u& b, const v4u& c) {
  float t0 = (asf(a.x) + asf(b.x)) + asf(c.x), t1 = (asf(a.z) + asf(b.z)) + asf(c.z);
  t0 += dpp<ROR4>(t0); t1 += dpp<ROR4>(t1);
  t0 += dpp<ROR8>(t0); t1 += dpp<ROR8>(t1);
  return rb(sum_rows_xy(t0, t1));
}

constexpr int L_RED = 0;
constexpr int L_RED2 = 16 * 192;
constexpr int L_Q = L_RED2 + 16 * 128;
constexpr int L_GU = L_Q + 96;
constexpr int L_WM = L_GU + 64;
constexpr int L_DT = L_WM + 16;
constexpr int L_END = 21504;

__global__ void __launch_bounds__(512, 2)
fused_decode(const u16* x_, const u16* kc_, const u16* vc_, const u16* wq_, const u16* wo_, const u16* wg_, const u16* wu_,
             const u16* wd_, u16* out_, unsigned char* ws_) {
  __shared__ __attribute__((aligned(16))) float lds[L_END];
  const int b = blockIdx.x, xg = b & 7, idx = b >> 3, h = 4 * xg + (idx & 3), s = idx >> 2;
  const int tid = threadIdx.x, lane = tid & 63, w = __builtin_amdgcn_readfirstlane(tid >> 6);
  const int u = lane >> 3, c8 = lane & 7, g = lane >> 4, n16 = lane & 15;
  v16u ka; v4u kb; u64 ts64;
  asm volatile("s_load_dwordx16 %0, %3, 0x0\n\ts_load_dwordx4 %1, %3, 0x40\n\ts_memrealtime %2"
               : "=&s"(ka), "=&s"(kb), "=&s"(ts64) : "s"(__builtin_amdgcn_kernarg_segment_ptr()));
  PIN();
  unsigned cpf0 = 0, cpf1 = 0;
  if (w == 0 && idx >= 30) {
    u64 pc;
    asm volatile("s_getpc_b64 %0" : "=s"(pc));
    const rsrc_t rc = mk((const void*)pc, CODEPF);
    asm volatile("buffer_load_dword %0, %2, %3, 0 offen\n\tbuffer_load_dword %1, %2, %3, %4 offen"
                 : "+v"(cpf0), "+v"(cpf1) : "v"(lane * 128), "s"(rc), "s"(8192));
  }
  PIN();
  const int rgl = idx / 5, cb = idx - 5 * rgl;
  const bool tile = idx < 30;
  int astride = rgl < 4 ? NH * DH * 2 : FF * 2;
  int aoff = 128 * cb * astride + (rgl < 4 ? 1536 * xg + 384 * rgl : 384 * xg);
  int abytes = tile ? 127 * astride + 384 : 0;
  int asel = rgl < 4 ? 0 : rgl - 3;
  int xro = (128 * cb + 16 * w + 2 * u) * 2;
  int ao = (16 * w + 2 * u) * astride + c8 * 16;
  unsigned kvoff = ((unsigned)h * SK + (SK - WIN)) * (DH * 2);
  int kvo = u * 384 + c8 * 16, kvs = w * 3072 + s * 24576;
  asm volatile("" : "+s"(astride), "+s"(aoff), "+s"(abytes), "+s"(asel), "+s"(kvoff), "+s"(kvs), "+v"(xro), "+v"(ao), "+v"(kvo));
  PIN();
  asm volatile("s_waitcnt lgkmcnt(0)" : "+s"(ka), "+s"(kb), "+s"(ts64));
  const unsigned ts = (unsigned)ts64 + EARLY;
#define SLOT0(n) not_before(ts + (((n) * TAU) >> 4))
#define KARG(lo, hi) (((u64)(hi) << 32) | (lo))
  const u16 *x = (const u16*)KARG(ka[0], ka[1]), *kc = (const u16*)KARG(ka[2], ka[3]), *vc = (const u16*)KARG(ka[4], ka[5]);
  const u16 *wq = (const u16*)KARG(ka[6], ka[7]), *wo = (const u16*)KARG(ka[8], ka[9]), *wg = (const u16*)KARG(ka[10], ka[11]);
  const u16 *wu = (const u16*)KARG(ka[12], ka[13]), *wd = (const u16*)KARG(ka[14], ka[15]);
  u16* out = (u16*)KARG(kb[0], kb[1]);
  unsigned char* ws = (unsigned char*)KARG(kb[2], kb[3]);

  const rsrc_t ra = mk((const char*)(asel == 0 ? wq : (asel == 1 ? wg : wu)) + aoff, abytes);
  v4u a[6];
#pragma unroll
  for (int t = 0; t < 3; ++t) a[t] = ld16<AUX_NT>(ra, ao + t * 128);
  PIN();
  const rsrc_t rx = mk(x, D * 2);
  const v4u xs = ld16<0>(rx, lane * 16);
  const unsigned xs4 = __builtin_amdgcn_raw_buffer_load_b32(rx, lane * 4 + 1024, 0, 0);
  const unsigned xr = __builtin_amdgcn_raw_buffer_load_b32(rx, xro, 0, 0);
  PIN();
#pragma unroll
  for (int t = 0; t < 3; ++t) a[3 + t] = ld16<AUX_NT>(ra, ao + t * 128, astride);
  PIN();
  const rsrc_t rws = mk(ws, WS_END);
  const unsigned epoch = __builtin_amdgcn_raw_buffer_load_b32(rws, WS_EPOCH, 0, AUX_SC1);
  const rsrc_t rk = mk((const char*)kc + kvoff, WIN * DH * 2), rv = mk((const char*)vc + kvoff, WIN * DH * 2);
  lockstep();
  v4u kv[48];
#define ISSUE(n) do { _Pragma("unroll") for (int t_ = 0; t_ < 3; ++t_) { \
    kv[3 * (n) + t_] = ld16<AUX_NT>((n) < 8 ? rk : rv, kvo + t_ * 128, kvs + (((n) + h) & 7) * 196608); PIN(); } } while (0)
  ISSUE(0);
  v4u mask;
  {
    const unsigned hm = (lane & 1) ? 0xffff0000u : 0x0000ffffu;
    const int r = c8 >> 1;
    mask = v4u{r == 0 ? hm : 0u, r == 1 ? hm : 0u, r == 2 ? hm : 0u, r == 3 ? hm : 0u};
  }
  pace<10>();
  ISSUE(1);
  float xn0, xn1;
  {
    const unsigned xv[5] = {xs.x, xs.y, xs.z, xs.w, xs4};
    float sum = 0.f;
#pragma unroll
    for (int j = 0; j < 5; ++j) sum += bflo(xv[j]) + bfhi(xv[j]);
    const float mean = wave_sum(sum) * (1.f / D);
    float sq = 0.f;
#pragma unroll
    for (int j = 0; j < 5; ++j) { const float d0 = bflo(xv[j]) - mean, d1 = bfhi(xv[j]) - mean; sq += d0 * d0 + d1 * d1; }
    const float var = rb(wave_sum(sq) * (1.f / D));
    const float mb = rb(mean), sd = rb(sqrtf(rb(var + 1e-5f)));
    xn0 = rb(bflo(xr) - mb) / sd;
    xn1 = rb(bfhi(xr) - mb) / sd;
  }
  asm volatile("" : "+v"(xn0) : "v"(cpf0), "v"(cpf1));
  const bool valid = ((lane >> 3) & 1) == (lane >> 5);
  float* const redp = lds + L_RED + (w * 2 + (lane >> 5)) * 192 + 8 * c8 + 4 * (g & 1);
  {
    const v4u d0 = diag(xn0, mask), d1 = diag(xn1, mask);
    f32x4 acc[3];
#pragma unroll
    for (int t = 0; t < 3; ++t) acc[t] = mfma(d0, a[t], f32x4{0.f, 0.f, 0.f, 0.f});
#pragma unroll
    for (int t = 0; t < 3; ++t) acc[t] = mfma(d1, a[3 + t], acc[t]);
    if (valid) {
#pragma unroll
      for (int t = 0; t < 3; ++t) *(f32x4*)(redp + 64 * t) = acc[t];
    }
    PIN();
  }
  ISSUE(2);
  const unsigned tag = (unsigned)__builtin_amdgcn_readfirstlane((int)epoch) * 0x9E3779B1u + 0x7F4A7C15u;
  wg_barrier();
  const float* const sump = lds + L_RED + 4 * g * 192 + 32 * w + 2 * n16;
#define SUM192() sum_rows2((*(const f32x2*)sump + *(const f32x2*)(sump + 192)) + (*(const f32x2*)(sump + 384) + *(const f32x2*)(sump + 576)))
  unsigned tref;
  {
    f32x2 sum = SUM192();
    tref = clock_lo();
    if (w == 6) { sum.x = asf(tref); sum.y = asf((unsigned)ts64); }
    publish(rws, !tile ? OOB : ((w < 6 && lane < 16) ? WS_QP + (xg * 30 + idx) * P_STRIDE + (16 * w + lane) * 16
                                                     : ((tid == 384 && cb == 2) ? WS_T0 + (rgl * 8 + xg) * 16 : OOB)), sum, tag);
  }
  PIN();
  const int yb = (cb * 48 + 6 * xg + rgl) * Y_STRIDE;
  const int ob = WS_OP + (h * 8 + s) * P_STRIDE;
  const rsrc_t rd = mk(wd + (size_t)(32 * (6 * xg + rgl)) * D + 128 * cb, tile ? 31 * D * 2 + 256 : 0);
  const rsrc_t ro = mk(wo + (size_t)(128 * (6 * xg + rgl)) * D + 128 * cb, tile ? 127 * D * 2 + 256 : 0);
  const bool glive = w < 6 || tile;
  const int gb = WS_QP + (xg * 30 + 5 * (w < 6 ? (idx & 3) : w - 2)) * P_STRIDE + (16 * (w < 6 ? w : rgl) + n16) * 16;
  const int go0 = glive ? gb + g * P_STRIDE : OOB;
  const int wl = 48 * w + lane - 16;
  const int go1 = g == 0 ? (glive ? gb + 4 * P_STRIDE : OOB)
                         : (wl < 96 ? ob + wl * 16
                                    : (wl < 224 ? (tile ? (wl < 160 ? WS_YP - 96 * 16 : WS_ZP - 160 * 16) + yb + wl * 16 : OOB)
                                                : (w == 7 ? WS_T0 + (lane - 16) * 16 : OOB)));
  pace<7>(); SLOT0(3); ISSUE(3); lockstep();
  pace<7>(); SLOT0(4); ISSUE(4); lockstep();
  pace<7>(); SLOT0(5);
  v4u g0 = ld16<AUX_SC1>(rws, go0), g1 = ld16<AUX_SC1>(rws, go1);
  lockstep();
  ISSUE(5); lockstep();
  pace<8>(); SLOT0(6); ISSUE(6); lockstep();
  pace<6>(); SLOT0(7); ISSUE(7);
#define GATHER_OK() (!glive || (missing(g0, tag) | (missing(g1, tag) & 0xffff)) == 0)
  if (__builtin_expect(!GATHER_OK(), 0)) {
    unsigned spins = 0;
#pragma unroll 1
    do {
      __builtin_amdgcn_s_sleep(2);
      reload2(g0, g1, rws, go0, go1);
    } while (!GATHER_OK() && ++spins < SPIN_CAP);
  }
  float* const red2p = lds + L_RED2 + w * 128 + 8 * n16 + 4 * (g & 1);
  {
    const f32x2 q = sum_rows2(f32x2{asf(g0.x), asf(g0.z)}) + f32x2{asf(g1.x), asf(g1.z)};
    if (lane < 16) {
      if (w < 6) ((unsigned*)(lds + L_Q))[w * 16 + lane] = pk(q.x, q.y);
      else *(f32x2*)(lds + L_GU + (w - 6) * 32 + 2 * lane) = q;
    }
    if (w == 7) {
      const bool have = g != 0 && tagged(g1, tag);
      int e = have ? (int)(tref - g1.x) : 0;
      e = e < -400 ? -400 : (e > 400 ? 400 : e);
      const float cnt = wave_sum(have ? 1.f : 0.f), em = wave_sum((float)e) * __builtin_amdgcn_rcpf(fmaxf(cnt, 1.f));
      if (lane == 0) lds[L_DT] = em;
      { const float st = (float)(int)(g1.z - (unsigned)ts64), tb = (float)(int)(g1.x - (unsigned)ts64);
        const float smax = max_u(max8(have ? st : -1e9f)), smin = -max_u(max8(have ? -st : -1e9f));
        const float smean = wave_sum(have ? st : 0.f) / cnt, bmean = wave_sum(have ? tb : 0.f) / cnt, bmax = max_u(max8(have ? tb : -1e9f));
        if (lane == 0) lds[L_DT + 1] = smean - smin; }
    }
  }
  wg_barrier();
  float sc[8];
  v4u qk[3];
#pragma unroll
  for (int t = 0; t < 3; ++t) qk[t] = *(const v4u*)(lds + L_Q + 32 * t + 4 * c8);
  tref += LEAD - (unsigned)__builtin_amdgcn_readfirstlane((int)lds[L_DT]);
  float wmax = -INFINITY;
#define QK(i) do { float d_[3]; \
    _Pragma("unroll") for (int t_ = 0; t_ < 3; ++t_) { d_[t_] = 0.f; _Pragma("unroll") for (int c_ = 0; c_ < 4; ++c_) d_[t_] = dot2(kv[3 * (i) + t_][c_], qk[t_][c_], d_[t_]); } \
    sc[i] = rb(rb(sum8((d_[0] + d_[1]) + d_[2])) * 0.07216878364870322f); wmax = fmaxf(wmax, sc[i]); PIN(); } while (0)
#define SLOT(n) not_before(tref + (((n) * TAU) >> 4))
  QK(0);
  pace<6>(); SLOT(8); ISSUE(8);
  QK(1); QK(2); QK(3);
  lockstep();
  pace<6>(); SLOT(9); ISSUE(9);
  QK(4); QK(5); QK(6);
  lockstep();
  pace<6>(); SLOT(10); ISSUE(10);
  QK(7);
  constexpr float LOG2E = 1.4426950408889634f;
  {
    wmax = max_u(wmax);
    float lsum = 0.f;
#pragma unroll
    for (int i = 0; i < 8; ++i) lsum += __builtin_amdgcn_exp2f((sc[i] - wmax) * LOG2E);
    lsum = sum_u(lsum);
    if (lane == 0) *(f32x2*)(lds + L_WM + 2 * w) = f32x2{wmax, lsum};
  }
  wg_barrier();
  {
    const f32x2 wm = *(const f32x2*)(lds + L_WM + 2 * c8);
    const float M = max8(wm.x);
    publish(rws, tid == 384 ? WS_ML + (h * 8 + s) * 16 : OOB, f32x2{M, sum8(__builtin_amdgcn_exp2f((wm.x - M) * LOG2E) * wm.y)}, tag);
    PIN();
  }
  pace<7>(); SLOT(11); ISSUE(11); lockstep();
  pace<7>(); SLOT(12); ISSUE(12);
  unsigned hp;
  {
    const float ga = lds[L_GU + 4 * w + g], up = lds[L_GU + 32 + 4 * w + g];
    const float hv = rb(gelu_tanh(rb(ga))) * rb(up);
    hp = pk(hv, hv);
    asm volatile("" : "+v"(hp));
  }
  lockstep();
  pace<7>(); SLOT(13); ISSUE(13); lockstep();
  pace<6>(); SLOT(14);
  const int mo = WS_ML + (h * 8 + c8) * 16;
  v4u ml = ld16<AUX_SC1>(rws, mo);
  PIN();
  ISSUE(14); lockstep();
  pace<7>(); SLOT(15); ISSUE(15);
  const int gd = 128 * rgl + 16 * w, hl = (gd * 683) >> 17;
  int o0 = WS_OP + ((4 * xg + hl) * 8 + u) * P_STRIDE + ((gd - 192 * hl) / 2 + c8) * 16;
  int ost = lane < 16 ? ob + (16 * w + lane) * 16 : OOB, yst = WS_YP + yb + lane * 16;
  asm volatile("" : "+v"(o0), "+v"(ost), "+v"(yst));
  lockstep();
  if (__builtin_expect(missing(ml, tag) != 0, 0)) {
    unsigned spins = 0;
#pragma unroll 1
    do { reload1(ml, rws, mo); } while (missing(ml, tag) != 0 && ++spins < SPIN_CAP);
  }
  {
    const float ms = asf(ml.x), M = max8(ms);
    const float inv = 1.f / sum8(__builtin_amdgcn_exp2f((ms - M) * LOG2E) * asf(ml.z));
#pragma unroll
    for (int i = 0; i < 8; ++i) sc[i] = __builtin_amdgcn_exp2f((sc[i] - M) * LOG2E) * inv;
  }
  f32x4 acc[3] = {f32x4{0.f, 0.f, 0.f, 0.f}, f32x4{0.f, 0.f, 0.f, 0.f}, f32x4{0.f, 0.f, 0.f, 0.f}};
#define PV(i) do { const v4u dp_ = diag(sc[i], mask); \
    _Pragma("unroll") for (int t_ = 0; t_ < 3; ++t_) acc[t_] = mfma(dp_, kv[24 + 3 * (i) + t_], acc[t_]); PIN(); } while (0)
#pragma unroll
  for (int i = 0; i < 4; ++i) PV(i);
  SLOT(16);
  const v4u wdr = ld16<AUX_NT>(rd, (4 * w + g) * 1280 + n16 * 16);
  PIN();
  v4u wor[4];
#pragma unroll
  for (int r = 0; r < 4; ++r) wor[r] = ld16<AUX_NT>(ro, (16 * w + 8 * (g >> 1) + (g & 1)) * 1280 + n16 * 16, r * 2560);
  PIN();
#pragma unroll
  for (int i = 4; i < 8; ++i) PV(i);
  if (valid) {
#pragma unroll
    for (int t = 0; t < 3; ++t) *(f32x4*)(redp + 64 * t) = acc[t];
  }
  wg_barrier();
  if (w < 6) publish(rws, ost, SUM192(), tag);
  const float* const sum2p = lds + L_RED2 + (lane >> 5) * 512 + 4 * (lane & 31);
#define SUM128(o) ({ const f32x4 s_ = (*(const f32x4*)(sum2p + (o)) + *(const f32x4*)(sum2p + (o) + 128)) + (*(const f32x4*)(sum2p + (o) + 256) + *(const f32x4*)(sum2p + (o) + 384)); \
    const auto a_ = __builtin_amdgcn_permlane32_swap(asu(s_[0]), asu(s_[2]), false, false), b_ = __builtin_amdgcn_permlane32_swap(asu(s_[1]), asu(s_[3]), false, false); \
    f32x2{asf(a_[0]), asf(b_[0])} + f32x2{asf(a_[1]), asf(b_[1])}; })
  unsigned spins = 0;
  if (tile) {
    const f32x4 z = mfma(v4u{hp, hp, hp, hp} & mask, wdr, f32x4{0.f, 0.f, 0.f, 0.f});
    if (lane < 32) *(f32x4*)(red2p + 1024) = z;
    wg_barrier();
    if (w == 7) publish(rws, yst + (WS_ZP - WS_YP), SUM128(1024), tag);
    if (w != 6) pace<1>(); else pace<0>();
    v4u e0;
#pragma unroll 1
    for (;;) {
      reload1(e0, rws, o0);
      if (missing(e0, tag) == 0) break;
      if (++spins >= SPIN_CAP) break;
    }
    float t0 = asf(e0.x), t1 = asf(e0.z);
    t0 += dpp<ROR8>(t0); t1 += dpp<ROR8>(t1);
    const float ov = sum_rows_xy(t0, t1);
    unsigned op = pk(ov, ov);
    op = (unsigned)__builtin_amdgcn_update_dpp((int)op, (int)op, ROR4, 0xc, 0xf, false);
    f32x4 y = mfma(diag_lane<0>(op, mask), wor[0], f32x4{0.f, 0.f, 0.f, 0.f});
    y = mfma(diag_lane<1>(op, mask), wor[1], y);
    y = mfma(diag_lane<2>(op, mask), wor[2], y);
    y = mfma(diag_lane<3>(op, mask), wor[3], y);
    if (lane < 32) *(f32x4*)red2p = y;
    wg_barrier();
    if (w == 0) publish(rws, yst, SUM128(0), tag);
  } else if (w >= 3) {
    const int unit = 5 * (xg + 8 * (idx - 30)) + w - 3;
    const int slot = 4 * (unit & 15) + (lane & 3);
    const int col = 128 * (unit >> 4) + 4 * (slot & 31) + 2 * (slot >> 5) + (g & 1);
    const int fo = ((unit >> 4) * 48 + (lane >> 2)) * Y_STRIDE + slot * 16;
    float xo = bflo(__builtin_amdgcn_raw_buffer_load_b16(rx, col * 2, 0, 0));
    v4u f0, f1, f2;
#pragma unroll 1
    do {
      reload3(f0, f1, f2, rws, WS_ZP + fo, WS_ZP + fo + 16 * Y_STRIDE, WS_ZP + fo + 32 * Y_STRIDE);
    } while ((missing(f0, tag) | missing(f1, tag) | missing(f2, tag)) != 0 && ++spins < SPIN_CAP);
    const float zz = colsum3(f0, f1, f2);
    float gt = rb(zz / (1.f + __builtin_amdgcn_exp2f(-zz * LOG2E)));
    int oc = (lane < 32 && n16 < 4) ? col * 2 : OOB;
    int y0 = WS_YP + fo, y1 = y0 + 16 * Y_STRIDE, y2 = y0 + 32 * Y_STRIDE;
    asm volatile("" : "+v"(gt), "+v"(xo), "+v"(oc), "+v"(y0), "+v"(y1), "+v"(y2));
    const rsrc_t rout = mk(out, D * 2);
#pragma unroll 1
    for (;;) {
      reload3(f0, f1, f2, rws, y0, y1, y2);
      if ((missing(f0, tag) | missing(f1, tag) | missing(f2, tag)) == 0) break;
      if (++spins >= SPIN_CAP) break;
    }
    __builtin_amdgcn_raw_buffer_store_b16((u16)pk(xo + rb(colsum3(f0, f1, f2) * gt), 0.f), rout, oc, 0, AUX_SC1);
    if (unit == 0) __builtin_amdgcn_raw_buffer_store_b32(tag, rws, lane == 0 ? WS_EPOCH : OOB, 0, AUX_SC1);
    { int v = 20 * (int)fminf(fmaxf(lds[L_DT + 1], 0.f), 1000.f); asm volatile("" : "+v"(v)); not_before(clock_lo() + (unsigned)__builtin_amdgcn_readfirstlane(v)); }
  }
}

extern "C" void kernel_launch(void* const* d_in, const int* in_sizes, int n_in, void* d_out, int out_size, void* d_ws, size_t ws_size,
                              hipStream_t stream) {
  fused_decode<<<256, 512, 0, stream>>>((const u16*)d_in[0], (const u16*)d_in[1], (const u16*)d_in[2], (const u16*)d_in[3],
                                        (const u16*)d_in[4], (const u16*)d_in[6], (const u16*)d_in[7], (const u16*)d_in[8],
                                        (u16*)d_out, (unsigned char*)d_ws);
}
```

```cpp
#include <hip/hip_runtime.h>
#include <hip/hip_bf16.h>

typedef unsigned short u16;
typedef unsigned v4u __attribute__((ext_vector_type(4)));
typedef unsigned v16u __attribute__((ext_vector_type(16)));
typedef float f32x4 __attribute__((ext_vector_type(4)));
typedef float f32x2 __attribute__((ext_vector_type(2)));
typedef __bf16 bf16x8 __attribute__((ext_vector_type(8)));
typedef __bf16 bf16x2 __attribute__((ext_vector_type(2)));
typedef __amdgpu_buffer_rsrc_t rsrc_t;
typedef unsigned long long u64;

constexpr int D = 640, NH = 32, DH = 192, SK = 32768, WIN = 4096, FF = 1536;
constexpr int AUX_NT = 2, AUX_SC1 = 16;
constexpr int OOB = 0x40000000;
constexpr unsigned SPIN_CAP = 1u << 16;

constexpr int WS_EPOCH = 0;
constexpr int P_STRIDE = 1664, Y_STRIDE = 1152;
constexpr int WS_T0 = 2048;
constexpr int WS_ML = 4096;
constexpr int WS_QP = 8192;
constexpr int WS_OP = WS_QP + 240 * P_STRIDE;
constexpr int WS_ZP = WS_OP + 256 * P_STRIDE;
constexpr int WS_YP = WS_ZP + 240 * Y_STRIDE;
constexpr int WS_END = WS_YP + 240 * Y_STRIDE;

#define TAU 1376
#define EARLY 150
#define LEAD -158
#define CODEPF 9300

__device__ __forceinline__ rsrc_t mk(const void* p, int bytes) { return __builtin_amdgcn_make_buffer_rsrc((void*)p, 0, bytes, 0x00020000); }
template <int AUX> __device__ __forceinline__ v4u ld16(rsrc_t r, int voff, int soff = 0) { return __builtin_amdgcn_raw_buffer_load_b128(r, voff, soff, AUX); }
__device__ __forceinline__ float asf(unsigned u) { return __builtin_bit_cast(float, u); }
__device__ __forceinline__ unsigned asu(float f) { return __builtin_bit_cast(unsigned, f); }
__device__ __forceinline__ void publish(rsrc_t r, int voff, f32x2 v, unsigned tag) {
  __builtin_amdgcn_raw_buffer_store_b128(v4u{asu(v.x), tag, asu(v.y), tag}, r, voff, 0, AUX_SC1);
}
__device__ __forceinline__ float bflo(unsigned u) { return asf(u << 16); }
__device__ __forceinline__ float bfhi(unsigned u) { return asf(u & 0xffff0000u); }
__device__ __forceinline__ unsigned pk(float lo, float hi) {
  bf16x2 r = {(__bf16)lo, (__bf16)hi};
  return __builtin_bit_cast(unsigned, r);
}
__device__ __forceinline__ float rb(float f) { return (float)(__bf16)f; }
__device__ __forceinline__ float dot2(unsigned a, unsigned b, float c) {
  return __builtin_amdgcn_fdot2_f32_bf16(__builtin_bit_cast(bf16x2, a), __builtin_bit_cast(bf16x2, b), c, false);
}
template <int CTRL> __device__ __forceinline__ float dpp(float x) {
  return asf((unsigned)__builtin_amdgcn_mov_dpp((int)asu(x), CTRL, 0xf, 0xf, true));
}
constexpr int XOR1 = 0xB1, XOR2 = 0x4E, HMIR = 0x141, ROR8 = 0x128, ROR4 = 0x124, BCAST = 0x150;
__device__ __forceinline__ float sum8(float x) { x += dpp<XOR1>(x); x += dpp<XOR2>(x); x += dpp<HMIR>(x); return x; }
__device__ __forceinline__ float max8(float x) { x = fmaxf(x, dpp<XOR1>(x)); x = fmaxf(x, dpp<XOR2>(x)); x = fmaxf(x, dpp<HMIR>(x)); return x; }
__device__ __forceinline__ float sum_rows(float x) {
  auto s = __builtin_amdgcn_permlane16_swap(asu(x), asu(x), false, false);
  x = asf(s[0]) + asf(s[1]);
  auto t = __builtin_amdgcn_permlane32_swap(asu(x), asu(x), false, false);
  return asf(t[0]) + asf(t[1]);
}
__device__ __forceinline__ float max_rows(float x) {
  auto s = __builtin_amdgcn_permlane16_swap(asu(x), asu(x), false, false);
  x = fmaxf(asf(s[0]), asf(s[1]));
  auto t = __builtin_amdgcn_permlane32_swap(asu(x), asu(x), false, false);
  return fmaxf(asf(t[0]), asf(t[1]));
}
__device__ __forceinline__ float sum_u(float x) { x += dpp<ROR8>(x); return sum_rows(x); }
__device__ __forceinline__ float max_u(float x) { x = fmaxf(x, dpp<ROR8>(x)); return max_rows(x); }
__device__ __forceinline__ float wave_sum(float x) { return sum_u(sum8(x)); }
__device__ __forceinline__ float sum_rows_xy(float x, float y) {
  auto s = __builtin_amdgcn_permlane16_swap(asu(x), asu(y), false, false);
  const float t = asf(s[0]) + asf(s[1]);
  auto r = __builtin_amdgcn_permlane32_swap(asu(t), asu(t), false, false);
  return asf(r[0]) + asf(r[1]);
}
__device__ __forceinline__ f32x2 sum_rows2(f32x2 v) {
  auto a = __builtin_amdgcn_permlane16_swap(asu(v.x), asu(v.x), false, false), b = __builtin_amdgcn_permlane16_swap(asu(v.y), asu(v.y), false, false);
  v = f32x2{asf(a[0]), asf(b[0])} + f32x2{asf(a[1]), asf(b[1])};
  auto c = __builtin_amdgcn_permlane32_swap(asu(v.x), asu(v.x), false, false), d = __builtin_amdgcn_permlane32_swap(asu(v.y), asu(v.y), false, false);
  return f32x2{asf(c[0]), asf(d[0])} + f32x2{asf(c[1]), asf(d[1])};
}
__device__ __forceinline__ v4u diag(float val, const v4u& mask) { const unsigned p = pk(val, val); return v4u{p, p, p, p} & mask; }
template <int L> __device__ __forceinline__ v4u diag_lane(unsigned p, const v4u& mask) {
  const unsigned d = (unsigned)__builtin_amdgcn_mov_dpp((int)p, BCAST + L, 0xf, 0xf, true);
  return v4u{d, d, d, d} & mask;
}
__device__ __forceinline__ f32x4 mfma(v4u a, v4u b, f32x4 c) {
  return __builtin_amdgcn_mfma_f32_16x16x32_bf16(__builtin_bit_cast(bf16x8, a), __builtin_bit_cast(bf16x8, b), c, 0, 0, 0);
}
#define PIN() __builtin_amdgcn_sched_barrier(0)
__device__ __forceinline__ void wg_barrier() {
  asm volatile("s_waitcnt lgkmcnt(0)" ::: "memory");
  __builtin_amdgcn_s_barrier();
  asm volatile("" ::: "memory");
}
__device__ __forceinline__ void lockstep() { PIN(); __builtin_amdgcn_s_barrier(); PIN(); }
template <int N> __device__ __forceinline__ void pace() { PIN(); asm volatile("s_waitcnt vmcnt(%0)" :: "i"(N)); PIN(); }
__device__ __forceinline__ unsigned clock_lo() {
  u64 t;
  asm volatile("s_memrealtime %0\n\ts_waitcnt lgkmcnt(0)" : "=s"(t) :: "memory");
  return (unsigned)t;
}
__device__ __forceinline__ void not_before(unsigned t) {
  PIN();
  asm volatile("1:\n\ts_memrealtime vcc\n\ts_waitcnt lgkmcnt(0)\n\ts_sub_u32 vcc_lo, vcc_lo, %0\n\ts_cmp_lt_i32 vcc_lo, 0\n\ts_cbranch_scc1 1b"
               :: "s"(t) : "vcc", "scc", "memory");
  PIN();
}
__device__ __forceinline__ bool tagged(const v4u& g, unsigned tag) { return (g.y == tag) & (g.w == tag); }
__device__ __forceinline__ u64 missing(const v4u& g, unsigned tag) { return __builtin_amdgcn_ballot_w64(g.y != tag) | __builtin_amdgcn_ballot_w64(g.w != tag); }
__device__ __forceinline__ void reload1(v4u& a, rsrc_t rs, int oa) {
  asm volatile("buffer_load_dwordx4 %0, %1, %2, 0 offen sc1\n\ts_waitcnt vmcnt(0)" : "=&v"(a) : "v"(oa), "s"(rs) : "memory");
}
__device__ __forceinline__ void reload2(v4u& a, v4u& b, rsrc_t rs, int oa, int ob) {
  asm volatile("buffer_load_dwordx4 %0, %2, %4, 0 offen sc1\n\tbuffer_load_dwordx4 %1, %3, %4, 0 offen sc1\n\ts_waitcnt vmcnt(0)"
               : "=&v"(a), "=&v"(b) : "v"(oa), "v"(ob), "s"(rs) : "memory");
}
__device__ __forceinline__ void reload3(v4u& a, v4u& b, v4u& c, rsrc_t rs, int oa, int ob, int oc) {
  asm volatile("buffer_load_dwordx4 %0, %3, %6, 0 offen sc1\n\tbuffer_load_dwordx4 %1, %4, %6, 0 offen sc1\n\t"
               "buffer_load_dwordx4 %2, %5, %6, 0 offen sc1\n\ts_waitcnt vmcnt(0)"
               : "=&v"(a), "=&v"(b), "=&v"(c) : "v"(oa), "v"(ob), "v"(oc), "s"(rs) : "memory");
}
__device__ __forceinline__ float gelu_tanh(float x) {
  const float u = 0.7978845608028654f * (x + 0.044715f * x * x * x);
  const float e = __builtin_amdgcn_exp2f(u * 2.885390081777927f);
  const float th = 1.f - 2.f / (e + 1.f);
  return 0.5f * x * (1.f + th);
}
__device__ __forceinline__ float colsum3(const v4u& a, const v4u& b, const v4u& c) {
  float t0 = (asf(a.x) + asf(b.x)) + asf(c.x), t1 = (asf(a.z) + asf(b.z)) + asf(c.z);
  t0 += dpp<ROR4>(t0); t1 += dpp<ROR4>(t1);
  t0 += dpp<ROR8>(t0); t1 += dpp<ROR8>(t1);
  return rb(sum_rows_xy(t0, t1));
}

constexpr int L_RED = 0;
constexpr int L_RED2 = 16 * 192;
constexpr int L_Q = L_RED2 + 16 * 128;
constexpr int L_GU = L_Q + 96;
constexpr int L_WM = L_GU + 64;
constexpr int L_DT = L_WM + 16;
constexpr int L_END = 21504;

__global__ void __launch_bounds__(512, 2)
fused_decode(const u16* x_, const u16* kc_, const u16* vc_, const u16* wq_, const u16* wo_, const u16* wg_, const u16* wu_,
             const u16* wd_, u16* out_, unsigned char* ws_) {
  __shared__ __attribute__((aligned(16))) float lds[L_END];
  const int b = blockIdx.x, xg = b & 7, idx = b >> 3, h = 4 * xg + (idx & 3), s = idx >> 2;
  const int tid = threadIdx.x, lane = tid & 63, w = __builtin_amdgcn_readfirstlane(tid >> 6);
  const int u = lane >> 3, c8 = lane & 7, g = lane >> 4, n16 = lane & 15;
  v16u ka; v4u kb; u64 ts64;
  asm volatile("s_load_dwordx16 %0, %3, 0x0\n\ts_load_dwordx4 %1, %3, 0x40\n\ts_memrealtime %2"
               : "=&s"(ka), "=&s"(kb), "=&s"(ts64) : "s"(__builtin_amdgcn_kernarg_segment_ptr()));
  asm volatile("s_waitcnt lgkmcnt(0)" : "+s"(ka), "+s"(kb), "+s"(ts64));
  const unsigned tk0 = clock_lo();
  PIN();
  unsigned cpf0 = 0, cpf1 = 0;
  if (w == 0 && idx >= 30) {
    u64 pc;
    asm volatile("s_getpc_b64 %0" : "=s"(pc));
    const rsrc_t rc = mk((const void*)pc, CODEPF);
    asm volatile("buffer_load_dword %0, %2, %3, 0 offen\n\tbuffer_load_dword %1, %2, %3, %4 offen"
                 : "+v"(cpf0), "+v"(cpf1) : "v"(lane * 128), "s"(rc), "s"(8192));
  }
  PIN();
  const int rgl = idx / 5, cb = idx - 5 * rgl;
  const bool tile = idx < 30;
  int astride = rgl < 4 ? NH * DH * 2 : FF * 2;
  int aoff = 128 * cb * astride + (rgl < 4 ? 1536 * xg + 384 * rgl : 384 * xg);
  int abytes = tile ? 127 * astride + 384 : 0;
  int asel = rgl < 4 ? 0 : rgl - 3;
  int xro = (128 * cb + 16 * w + 2 * u) * 2;
  int ao = (16 * w + 2 * u) * astride + c8 * 16;
  unsigned kvoff = ((unsigned)h * SK + (SK - WIN)) * (DH * 2);
  int kvo = u * 384 + c8 * 16, kvs = w * 3072 + s * 24576;
  asm volatile("" : "+s"(astride), "+s"(aoff), "+s"(abytes), "+s"(asel), "+s"(kvoff), "+s"(kvs), "+v"(xro), "+v"(ao), "+v"(kvo));
  PIN();
  asm volatile("s_waitcnt lgkmcnt(0)" : "+s"(ka), "+s"(kb), "+s"(ts64));
  const unsigned ts = (unsigned)ts64 + EARLY;
#define SLOT0(n) not_before(ts + (((n) * TAU) >> 4))
#define KARG(lo, hi) (((u64)(hi) << 32) | (lo))
  const u16 *x = (const u16*)KARG(ka[0], ka[1]), *kc = (const u16*)KARG(ka[2], ka[3]), *vc = (const u16*)KARG(ka[4], ka[5]);
  const u16 *wq = (const u16*)KARG(ka[6], ka[7]), *wo = (const u16*)KARG(ka[8], ka[9]), *wg = (const u16*)KARG(ka[10], ka[11]);
  const u16 *wu = (const u16*)KARG(ka[12], ka[13]), *wd = (const u16*)KARG(ka[14], ka[15]);
  u16* out = (u16*)KARG(kb[0], kb[1]);
  unsigned char* ws = (unsigned char*)KARG(kb[2], kb[3]);

  const rsrc_t ra = mk((const char*)(asel == 0 ? wq : (asel == 1 ? wg : wu)) + aoff, abytes);
  PIN();
  const unsigned tk = clock_lo();
  PIN();
  v4u a[6];
#pragma unroll
  for (int t = 0; t < 3; ++t) a[t] = ld16<AUX_NT>(ra, ao + t * 128);
  PIN();
  const rsrc_t rx = mk(x, D * 2);
  const v4u xs = ld16<0>(rx, lane * 16);
  const unsigned xs4 = __builtin_amdgcn_raw_buffer_load_b32(rx, lane * 4 + 1024, 0, 0);
  const unsigned xr = __builtin_amdgcn_raw_buffer_load_b32(rx, xro, 0, 0);
  PIN();
#pragma unroll
  for (int t = 0; t < 3; ++t) a[3 + t] = ld16<AUX_NT>(ra, ao + t * 128, astride);
  PIN();
  const rsrc_t rws = mk(ws, WS_END);
  const unsigned epoch = __builtin_amdgcn_raw_buffer_load_b32(rws, WS_EPOCH, 0, AUX_SC1);
  const rsrc_t rk = mk((const char*)kc + kvoff, WIN * DH * 2), rv = mk((const char*)vc + kvoff, WIN * DH * 2);
  lockstep();
  v4u kv[48];
#define ISSUE(n) do { _Pragma("unroll") for (int t_ = 0; t_ < 3; ++t_) { \
    kv[3 * (n) + t_] = ld16<AUX_NT>((n) < 8 ? rk : rv, kvo + t_ * 128, kvs + (((n) + h) & 7) * 196608); PIN(); } } while (0)
  ISSUE(0);
  v4u mask;
  {
    const unsigned hm = (lane & 1) ? 0xffff0000u : 0x0000ffffu;
    const int r = c8 >> 1;
    mask = v4u{r == 0 ? hm : 0u, r == 1 ? hm : 0u, r == 2 ? hm : 0u, r == 3 ? hm : 0u};
  }
  pace<10>();
  ISSUE(1);
  float xn0, xn1;
  {
    const unsigned xv[5] = {xs.x, xs.y, xs.z, xs.w, xs4};
    float sum = 0.f;
#pragma unroll
    for (int j = 0; j < 5; ++j) sum += bflo(xv[j]) + bfhi(xv[j]);
    const float mean = wave_sum(sum) * (1.f / D);
    float sq = 0.f;
#pragma unroll
    for (int j = 0; j < 5; ++j) { const float d0 = bflo(xv[j]) - mean, d1 = bfhi(xv[j]) - mean; sq += d0 * d0 + d1 * d1; }
    const float var = rb(wave_sum(sq) * (1.f / D));
    const float mb = rb(mean), sd = rb(sqrtf(rb(var + 1e-5f)));
    xn0 = rb(bflo(xr) - mb) / sd;
    xn1 = rb(bfhi(xr) - mb) / sd;
  }
  asm volatile("" : "+v"(xn0) : "v"(cpf0), "v"(cpf1));
  const bool valid = ((lane >> 3) & 1) == (lane >> 5);
  float* const redp = lds + L_RED + (w * 2 + (lane >> 5)) * 192 + 8 * c8 + 4 * (g & 1);
  {
    const v4u d0 = diag(xn0, mask), d1 = diag(xn1, mask);
    f32x4 acc[3];
#pragma unroll
    for (int t = 0; t < 3; ++t) acc[t] = mfma(d0, a[t], f32x4{0.f, 0.f, 0.f, 0.f});
#pragma unroll
    for (int t = 0; t < 3; ++t) acc[t] = mfma(d1, a[3 + t], acc[t]);
    if (valid) {
#pragma unroll
      for (int t = 0; t < 3; ++t) *(f32x4*)(redp + 64 * t) = acc[t];
    }
    PIN();
  }
  ISSUE(2);
  const unsigned tag = (unsigned)__builtin_amdgcn_readfirstlane((int)epoch) * 0x9E3779B1u + 0x7F4A7C15u;
  wg_barrier();
  const float* const sump = lds + L_RED + 4 * g * 192 + 32 * w + 2 * n16;
#define SUM192() sum_rows2((*(const f32x2*)sump + *(const f32x2*)(sump + 192)) + (*(const f32x2*)(sump + 384) + *(const f32x2*)(sump + 576)))
  unsigned tref;
  {
    f32x2 sum = SUM192();
    tref = clock_lo();
    if (w == 6) { sum.x = asf(tref); sum.y = asf(tref - (tk - tk0)); }
    publish(rws, !tile ? OOB : ((w < 6 && lane < 16) ? WS_QP + (xg * 30 + idx) * P_STRIDE + (16 * w + lane) * 16
                                                     : ((tid == 384 && cb == 2) ? WS_T0 + (rgl * 8 + xg) * 16 : OOB)), sum, tag);
  }
  PIN();
  const int yb = (cb * 48 + 6 * xg + rgl) * Y_STRIDE;
  const int ob = WS_OP + (h * 8 + s) * P_STRIDE;
  const rsrc_t rd = mk(wd + (size_t)(32 * (6 * xg + rgl)) * D + 128 * cb, tile ? 31 * D * 2 + 256 : 0);
  const rsrc_t ro = mk(wo + (size_t)(128 * (6 * xg + rgl)) * D + 128 * cb, tile ? 127 * D * 2 + 256 : 0);
  const bool glive = w < 6 || tile;
  const int gb = WS_QP + (xg * 30 + 5 * (w < 6 ? (idx & 3) : w - 2)) * P_STRIDE + (16 * (w < 6 ? w : rgl) + n16) * 16;
  const int go0 = glive ? gb + g * P_STRIDE : OOB;
  const int wl = 48 * w + lane - 16;
  const int go1 = g == 0 ? (glive ? gb + 4 * P_STRIDE : OOB)
                         : (wl < 96 ? ob + wl * 16
                                    : (wl < 224 ? (tile ? (wl < 160 ? WS_YP - 96 * 16 : WS_ZP - 160 * 16) + yb + wl * 16 : OOB)
                                                : (w == 7 ? WS_T0 + (lane - 16) * 16 : OOB)));
  pace<7>(); SLOT0(3); ISSUE(3); lockstep();
  pace<7>(); SLOT0(4); ISSUE(4); lockstep();
  pace<7>(); SLOT0(5);
  v4u g0 = ld16<AUX_SC1>(rws, go0), g1 = ld16<AUX_SC1>(rws, go1);
  lockstep();
  ISSUE(5); lockstep();
  pace<8>(); SLOT0(6); ISSUE(6); lockstep();
  pace<6>(); SLOT0(7); ISSUE(7);
#define GATHER_OK() (!glive || (missing(g0, tag) | (missing(g1, tag) & 0xffff)) == 0)
  if (__builtin_expect(!GATHER_OK(), 0)) {
    unsigned spins = 0;
#pragma unroll 1
    do {
      __builtin_amdgcn_s_sleep(2);
      reload2(g0, g1, rws, go0, go1);
    } while (!GATHER_OK() && ++spins < SPIN_CAP);
  }
  float* const red2p = lds + L_RED2 + w * 128 + 8 * n16 + 4 * (g & 1);
  {
    const f32x2 q = sum_rows2(f32x2{asf(g0.x), asf(g0.z)}) + f32x2{asf(g1.x), asf(g1.z)};
    if (lane < 16) {
      if (w < 6) ((unsigned*)(lds + L_Q))[w * 16 + lane] = pk(q.x, q.y);
      else *(f32x2*)(lds + L_GU + (w - 6) * 32 + 2 * lane) = q;
    }
    if (w == 7) {
      const bool have = g != 0 && tagged(g1, tag);
      int e = have ? (int)(tref - g1.x) : 0;
      e = e < -400 ? -400 : (e > 400 ? 400 : e);
      const float cnt = wave_sum(have ? 1.f : 0.f), em = wave_sum((float)e) * __builtin_amdgcn_rcpf(fmaxf(cnt, 1.f));
      if (lane == 0) lds[L_DT] = em;
      { const float st = (float)(int)(g1.z - (unsigned)ts64), tb = (float)(int)(g1.x - (unsigned)ts64);
        const float smax = max_u(max8(have ? st : -1e9f)), smin = -max_u(max8(have ? -st : -1e9f));
        const float smean = wave_sum(have ? st : 0.f) / cnt, bmean = wave_sum(have ? tb : 0.f) / cnt, bmax = max_u(max8(have ? tb : -1e9f));
        if (lane == 0) lds[L_DT + 1] = bmean - smean; }
    }
  }
  wg_barrier();
  float sc[8];
  v4u qk[3];
#pragma unroll
  for (int t = 0; t < 3; ++t) qk[t] = *(const v4u*)(lds + L_Q + 32 * t + 4 * c8);
  tref += LEAD - (unsigned)__builtin_amdgcn_readfirstlane((int)lds[L_DT]);
  float wmax = -INFINITY;
#define QK(i) do { float d_[3]; \
    _Pragma("unroll") for (int t_ = 0; t_ < 3; ++t_) { d_[t_] = 0.f; _Pragma("unroll") for (int c_ = 0; c_ < 4; ++c_) d_[t_] = dot2(kv[3 * (i) + t_][c_], qk[t_][c_], d_[t_]); } \
    sc[i] = rb(rb(sum8((d_[0] + d_[1]) + d_[2])) * 0.07216878364870322f); wmax = fmaxf(wmax, sc[i]); PIN(); } while (0)
#define SLOT(n) not_before(tref + (((n) * TAU) >> 4))
  QK(0);
  pace<6>(); SLOT(8); ISSUE(8);
  QK(1); QK(2); QK(3);
  lockstep();
  pace<6>(); SLOT(9); ISSUE(9);
  QK(4); QK(5); QK(6);
  lockstep();
  pace<6>(); SLOT(10); ISSUE(10);
  QK(7);
  constexpr float LOG2E = 1.4426950408889634f;
  {
    wmax = max_u(wmax);
    float lsum = 0.f;
#pragma unroll
    for (int i = 0; i < 8; ++i) lsum += __builtin_amdgcn_exp2f((sc[i] - wmax) * LOG2E);
    lsum = sum_u(lsum);
    if (lane == 0) *(f32x2*)(lds + L_WM + 2 * w) = f32x2{wmax, lsum};
  }
  wg_barrier();
  {
    const f32x2 wm = *(const f32x2*)(lds + L_WM + 2 * c8);
    const float M = max8(wm.x);
    publish(rws, tid == 384 ? WS_ML + (h * 8 + s) * 16 : OOB, f32x2{M, sum8(__builtin_amdgcn_exp2f((wm.x - M) * LOG2E) * wm.y)}, tag);
    PIN();
  }
  pace<7>(); SLOT(11); ISSUE(11); lockstep();
  pace<7>(); SLOT(12); ISSUE(12);
  unsigned hp;
  {
    const float ga = lds[L_GU + 4 * w + g], up = lds[L_GU + 32 + 4 * w + g];
    const float hv = rb(gelu_tanh(rb(ga))) * rb(up);
    hp = pk(hv, hv);
    asm volatile("" : "+v"(hp));
  }
  lockstep();
  pace<7>(); SLOT(13); ISSUE(13); lockstep();
  pace<6>(); SLOT(14);
  const int mo = WS_ML + (h * 8 + c8) * 16;
  v4u ml = ld16<AUX_SC1>(rws, mo);
  PIN();
  ISSUE(14); lockstep();
  pace<7>(); SLOT(15); ISSUE(15);
  const int gd = 128 * rgl + 16 * w, hl = (gd * 683) >> 17;
  int o0 = WS_OP + ((4 * xg + hl) * 8 + u) * P_STRIDE + ((gd - 192 * hl) / 2 + c8) * 16;
  int ost = lane < 16 ? ob + (16 * w + lane) * 16 : OOB, yst = WS_YP + yb + lane * 16;
  asm volatile("" : "+v"(o0), "+v"(ost), "+v"(yst));
  lockstep();
  if (__builtin_expect(missing(ml, tag) != 0, 0)) {
    unsigned spins = 0;
#pragma unroll 1
    do { reload1(ml, rws, mo); } while (missing(ml, tag) != 0 && ++spins < SPIN_CAP);
  }
  {
    const float ms = asf(ml.x), M = max8(ms);
    const float inv = 1.f / sum8(__builtin_amdgcn_exp2f((ms - M) * LOG2E) * asf(ml.z));
#pragma unroll
    for (int i = 0; i < 8; ++i) sc[i] = __builtin_amdgcn_exp2f((sc[i] - M) * LOG2E) * inv;
  }
  f32x4 acc[3] = {f32x4{0.f, 0.f, 0.f, 0.f}, f32x4{0.f, 0.f, 0.f, 0.f}, f32x4{0.f, 0.f, 0.f, 0.f}};
#define PV(i) do { const v4u dp_ = diag(sc[i], mask); \
    _Pragma("unroll") for (int t_ = 0; t_ < 3; ++t_) acc[t_] = mfma(dp_, kv[24 + 3 * (i) + t_], acc[t_]); PIN(); } while (0)
#pragma unroll
  for (int i = 0; i < 4; ++i) PV(i);
  SLOT(16);
  const v4u wdr = ld16<AUX_NT>(rd, (4 * w + g) * 1280 + n16 * 16);
  PIN();
  v4u wor[4];
#pragma unroll
  for (int r = 0; r < 4; ++r) wor[r] = ld16<AUX_NT>(ro, (16 * w + 8 * (g >> 1) + (g & 1)) * 1280 + n16 * 16, r * 2560);
  PIN();
#pragma unroll
  for (int i = 4; i < 8; ++i) PV(i);
  if (valid) {
#pragma unroll
    for (int t = 0; t < 3; ++t) *(f32x4*)(redp + 64 * t) = acc[t];
  }
  wg_barrier();
  if (w < 6) publish(rws, ost, SUM192(), tag);
  const float* const sum2p = lds + L_RED2 + (lane >> 5) * 512 + 4 * (lane & 31);
#define SUM128(o) ({ const f32x4 s_ = (*(const f32x4*)(sum2p + (o)) + *(const f32x4*)(sum2p + (o) + 128)) + (*(const f32x4*)(sum2p + (o) + 256) + *(const f32x4*)(sum2p + (o) + 384)); \
    const auto a_ = __builtin_amdgcn_permlane32_swap(asu(s_[0]), asu(s_[2]), false, false), b_ = __builtin_amdgcn_permlane32_swap(asu(s_[1]), asu(s_[3]), false, false); \
    f32x2{asf(a_[0]), asf(b_[0])} + f32x2{asf(a_[1]), asf(b_[1])}; })
  unsigned spins = 0;
  if (tile) {
    const f32x4 z = mfma(v4u{hp, hp, hp, hp} & mask, wdr, f32x4{0.f, 0.f, 0.f, 0.f});
    if (lane < 32) *(f32x4*)(red2p + 1024) = z;
    wg_barrier();
    if (w == 7) publish(rws, yst + (WS_ZP - WS_YP), SUM128(1024), tag);
    if (w != 6) pace<1>(); else pace<0>();
    v4u e0;
#pragma unroll 1
    for (;;) {
      reload1(e0, rws, o0);
      if (missing(e0, tag) == 0) break;
      if (++spins >= SPIN_CAP) break;
    }
    float t0 = asf(e0.x), t1 = asf(e0.z);
    t0 += dpp<ROR8>(t0); t1 += dpp<ROR8>(t1);
    const float ov = sum_rows_xy(t0, t1);
    unsigned op = pk(ov, ov);
    op = (unsigned)__builtin_amdgcn_update_dpp((int)op, (int)op, ROR4, 0xc, 0xf, false);
    f32x4 y = mfma(diag_lane<0>(op, mask), wor[0], f32x4{0.f, 0.f, 0.f, 0.f});
    y = mfma(diag_lane<1>(op, mask), wor[1], y);
    y = mfma(diag_lane<2>(op, mask), wor[2], y);
    y = mfma(diag_lane<3>(op, mask), wor[3], y);
    if (lane < 32) *(f32x4*)red2p = y;
    wg_barrier();
    if (w == 0) publish(rws, yst, SUM128(0), tag);
  } else if (w >= 3) {
    const int unit = 5 * (xg + 8 * (idx - 30)) + w - 3;
    const int slot = 4 * (unit & 15) + (lane & 3);
    const int col = 128 * (unit >> 4) + 4 * (slot & 31) + 2 * (slot >> 5) + (g & 1);
    const int fo = ((unit >> 4) * 48 + (lane >> 2)) * Y_STRIDE + slot * 16;
    float xo = bflo(__builtin_amdgcn_raw_buffer_load_b16(rx, col * 2, 0, 0));
    v4u f0, f1, f2;
#pragma unroll 1
    do {
      reload3(f0, f1, f2, rws, WS_ZP + fo, WS_ZP + fo + 16 * Y_STRIDE, WS_ZP + fo + 32 * Y_STRIDE);
    } while ((missing(f0, tag) | missing(f1, tag) | missing(f2, tag)) != 0 && ++spins < SPIN_CAP);
    const float zz = colsum3(f0, f1, f2);
    float gt = rb(zz / (1.f + __builtin_amdgcn_exp2f(-zz * LOG2E)));
    int oc = (lane < 32 && n16 < 4) ? col * 2 : OOB;
    int y0 = WS_YP + fo, y1 = y0 + 16 * Y_STRIDE, y2 = y0 + 32 * Y_STRIDE;
    asm volatile("" : "+v"(gt), "+v"(xo), "+v"(oc), "+v"(y0), "+v"(y1), "+v"(y2));
    const rsrc_t rout = mk(out, D * 2);
#pragma unroll 1
    for (;;) {
      reload3(f0, f1, f2, rws, y0, y1, y2);
      if ((missing(f0, tag) | missing(f1, tag) | missing(f2, tag)) == 0) break;
      if (++spins >= SPIN_CAP) break;
    }
    __builtin_amdgcn_raw_buffer_store_b16((u16)pk(xo + rb(colsum3(f0, f1, f2) * gt), 0.f), rout, oc, 0, AUX_SC1);
    if (unit == 0) __builtin_amdgcn_raw_buffer_store_b32(tag, rws, lane == 0 ? WS_EPOCH : OOB, 0, AUX_SC1);
    { int v = 20 * (int)fminf(fmaxf(lds[L_DT + 1], 0.f), 1000.f); asm volatile("" : "+v"(v)); not_before(clock_lo() + (unsigned)__builtin_amdgcn_readfirstlane(v)); }
  }
}

extern "C" void kernel_launch(void* const* d_in, const int* in_sizes, int n_in, void* d_out, int out_size, void* d_ws, size_t ws_size,
                              hipStream_t stream) {
  fused_decode<<<256, 512, 0, stream>>>((const u16*)d_in[0], (const u16*)d_in[1], (const u16*)d_in[2], (const u16*)d_in[3],
                                        (const u16*)d_in[4], (const u16*)d_in[6], (const u16*)d_in[7], (const u16*)d_in[8],
                                        (u16*)d_out, (unsigned char*)d_ws);
}
```

```cpp
#include <hip/hip_runtime.h>
#include <hip/hip_bf16.h>

typedef unsigned short u16;
typedef unsigned v4u __attribute__((ext_vector_type(4)));
typedef unsigned v16u __attribute__((ext_vector_type(16)));
typedef float f32x4 __attribute__((ext_vector_type(4)));
typedef float f32x2 __attribute__((ext_vector_type(2)));
typedef __bf16 bf16x8 __attribute__((ext_vector_type(8)));
typedef __bf16 bf16x2 __attribute__((ext_vector_type(2)));
typedef __amdgpu_buffer_rsrc_t rsrc_t;
typedef unsigned long long u64;

constexpr int D = 640, NH = 32, DH = 192, SK = 32768, WIN = 4096, FF = 1536;
constexpr int AUX_NT = 2, AUX_SC1 = 16;
constexpr int OOB = 0x40000000;
constexpr unsigned SPIN_CAP = 1u << 16;

constexpr int WS_EPOCH = 0;
constexpr int P_STRIDE = 1664, Y_STRIDE = 1152;
constexpr int WS_T0 = 2048;
constexpr int WS_ML = 4096;
constexpr int WS_QP = 8192;
constexpr int WS_OP = WS_QP + 240 * P_STRIDE;
constexpr int WS_ZP = WS_OP + 256 * P_STRIDE;
constexpr int WS_YP = WS_ZP + 240 * Y_STRIDE;
constexpr int WS_END = WS_YP + 240 * Y_STRIDE;

#define TAU 1376
#define EARLY 123
#define LEAD -158
#define CODEPF 9300

__device__ __forceinline__ rsrc_t mk(const void* p, int bytes) { return __builtin_amdgcn_make_buffer_rsrc((void*)p, 0, bytes, 0x00020000); }
template <int AUX> __device__ __forceinline__ v4u ld16(rsrc_t r, int voff, int soff = 0) { return __builtin_amdgcn_raw_buffer_load_b128(r, voff, soff, AUX); }
__device__ __forceinline__ float asf(unsigned u) { return __builtin_bit_cast(float, u); }
__device__ __forceinline__ unsigned asu(float f) { return __builtin_bit_cast(unsigned, f); }
__device__ __forceinline__ void publish(rsrc_t r, int voff, f32x2 v, unsigned tag) {
  __builtin_amdgcn_raw_buffer_store_b128(v4u{asu(v.x), tag, asu(v.y), tag}, r, voff, 0, AUX_SC1);
}
__device__ __forceinline__ float bflo(unsigned u) { return asf(u << 16); }
__device__ __forceinline__ float bfhi(unsigned u) { return asf(u & 0xffff0000u); }
__device__ __forceinline__ unsigned pk(float lo, float hi) {
  bf16x2 r = {(__bf16)lo, (__bf16)hi};
  return __builtin_bit_cast(unsigned, r);
}
__device__ __forceinline__ float rb(float f) { return (float)(__bf16)f; }
__device__ __forceinline__ float dot2(unsigned a, unsigned b, float c) {
  return __builtin_amdgcn_fdot2_f32_bf16(__builtin_bit_cast(bf16x2, a), __builtin_bit_cast(bf16x2, b), c, false);
}
template <int CTRL> __device__ __forceinline__ float dpp(float x) {
  return asf((unsigned)__builtin_amdgcn_mov_dpp((int)asu(x), CTRL, 0xf, 0xf, true));
}
constexpr int XOR1 = 0xB1, XOR2 = 0x4E, HMIR = 0x141, ROR8 = 0x128, ROR4 = 0x124, BCAST = 0x150;
__device__ __forceinline__ float sum8(float x) { x += dpp<XOR1>(x); x += dpp<XOR2>(x); x += dpp<HMIR>(x); return x; }
__device__ __forceinline__ float max8(float x) { x = fmaxf(x, dpp<XOR1>(x)); x = fmaxf(x, dpp<XOR2>(x)); x = fmaxf(x, dpp<HMIR>(x)); return x; }
__device__ __forceinline__ float sum_rows(float x) {
  auto s = __builtin_amdgcn_permlane16_swap(asu(x), asu(x), false, false);
  x = asf(s[0]) + asf(s[1]);
  auto t = __builtin_amdgcn_permlane32_swap(asu(x), asu(x), false, false);
  return asf(t[0]) + asf(t[1]);
}
__device__ __forceinline__ float max_rows(float x) {
  auto s = __builtin_amdgcn_permlane16_swap(asu(x), asu(x), false, false);
  x = fmaxf(asf(s[0]), asf(s[1]));
  auto t = __builtin_amdgcn_permlane32_swap(asu(x), asu(x), false, false);
  return fmaxf(asf(t[0]), asf(t[1]));
}
__device__ __forceinline__ float sum_u(float x) { x += dpp<ROR8>(x); return sum_rows(x); }
__device__ __forceinline__ float max_u(float x) { x = fmaxf(x, dpp<ROR8>(x)); return max_rows(x); }
__device__ __forceinline__ float wave_sum(float x) { return sum_u(sum8(x)); }
__device__ __forceinline__ float sum_rows_xy(float x, float y) {
  auto s = __builtin_amdgcn_permlane16_swap(asu(x), asu(y), false, false);
  const float t = asf(s[0]) + asf(s[1]);
  auto r = __builtin_amdgcn_permlane32_swap(asu(t), asu(t), false, false);
  return asf(r[0]) + asf(r[1]);
}
__device__ __forceinline__ f32x2 sum_rows2(f32x2 v) {
  auto a = __builtin_amdgcn_permlane16_swap(asu(v.x), asu(v.x), false, false), b = __builtin_amdgcn_permlane16_swap(asu(v.y), asu(v.y), false, false);
  v = f32x2{asf(a[0]), asf(b[0])} + f32x2{asf(a[1]), asf(b[1])};
  auto c = __builtin_amdgcn_permlane32_swap(asu(v.x), asu(v.x), false, false), d = __builtin_amdgcn_permlane32_swap(asu(v.y), asu(v.y), false, false);
  return f32x2{asf(c[0]), asf(d[0])} + f32x2{asf(c[1]), asf(d[1])};
}
__device__ __forceinline__ v4u diag(float val, const v4u& mask) { const unsigned p = pk(val, val); return v4u{p, p, p, p} & mask; }
template <int L> __device__ __forceinline__ v4u diag_lane(unsigned p, const v4u& mask) {
  const unsigned d = (unsigned)__builtin_amdgcn_mov_dpp((int)p, BCAST + L, 0xf, 0xf, true);
  return v4u{d, d, d, d} & mask;
}
__device__ __forceinline__ f32x4 mfma(v4u a, v4u b, f32x4 c) {
  return __builtin_amdgcn_mfma_f32_16x16x32_bf16(__builtin_bit_cast(bf16x8, a), __builtin_bit_cast(bf16x8, b), c, 0, 0, 0);
}
#define PIN() __builtin_amdgcn_sched_barrier(0)
__device__ __forceinline__ void wg_barrier() {
  asm volatile("s_waitcnt lgkmcnt(0)" ::: "memory");
  __builtin_amdgcn_s_barrier();
  asm volatile("" ::: "memory");
}
__device__ __forceinline__ void lockstep() { PIN(); __builtin_amdgcn_s_barrier(); PIN(); }
template <int N> __device__ __forceinline__ void pace() { PIN(); asm volatile("s_waitcnt vmcnt(%0)" :: "i"(N)); PIN(); }
__device__ __forceinline__ unsigned clock_lo() {
  u64 t;
  asm volatile("s_memrealtime %0\n\ts_waitcnt lgkmcnt(0)" : "=s"(t) :: "memory");
  return (unsigned)t;
}
__device__ __forceinline__ void not_before(unsigned t) {
  PIN();
  asm volatile("1:\n\ts_memrealtime vcc\n\ts_waitcnt lgkmcnt(0)\n\ts_sub_u32 vcc_lo, vcc_lo, %0\n\ts_cmp_lt_i32 vcc_lo, 0\n\ts_cbranch_scc1 1b"
               :: "s"(t) : "vcc", "scc", "memory");
  PIN();
}
__device__ __forceinline__ bool tagged(const v4u& g, unsigned tag) { return (g.y == tag) & (g.w == tag); }
__device__ __forceinline__ u64 missing(const v4u& g, unsigned tag) { return __builtin_amdgcn_ballot_w64(g.y != tag) | __builtin_amdgcn_ballot_w64(g.w != tag); }
__device__ __forceinline__ void reload1(v4u& a, rsrc_t rs, int oa) {
  asm volatile("buffer_load_dwordx4 %0, %1, %2, 0 offen sc1\n\ts_waitcnt vmcnt(0)" : "=&v"(a) : "v"(oa), "s"(rs) : "memory");
}
__device__ __forceinline__ void reload2(v4u& a, v4u& b, rsrc_t rs, int oa, int ob) {
  asm volatile("buffer_load_dwordx4 %0, %2, %4, 0 offen sc1\n\tbuffer_load_dwordx4 %1, %3, %4, 0 offen sc1\n\ts_waitcnt vmcnt(0)"
               : "=&v"(a), "=&v"(b) : "v"(oa), "v"(ob), "s"(rs) : "memory");
}
__device__ __forceinline__ void reload3(v4u& a, v4u& b, v4u& c, rsrc_t rs, int oa, int ob, int oc) {
  asm volatile("buffer_load_dwordx4 %0, %3, %6, 0 offen sc1\n\tbuffer_load_dwordx4 %1, %4, %6, 0 offen sc1\n\t"
               "buffer_load_dwordx4 %2, %5, %6, 0 offen sc1\n\ts_waitcnt vmcnt(0)"
               : "=&v"(a), "=&v"(b), "=&v"(c) : "v"(oa), "v"(ob), "v"(oc), "s"(rs) : "memory");
}
__device__ __forceinline__ float gelu_tanh(float x) {
  const float u = 0.7978845608028654f * (x + 0.044715f * x * x * x);
  const float e = __builtin_amdgcn_exp2f(u * 2.885390081777927f);
  const float th = 1.f - 2.f / (e + 1.f);
  return 0.5f * x * (1.f + th);
}
__device__ __forceinline__ float colsum3(const v4u& a, const v4u& b, const v4u& c) {
  float t0 = (asf(a.x) + asf(b.x)) + asf(c.x), t1 = (asf(a.z) + asf(b.z)) + asf(c.z);
  t0 += dpp<ROR4>(t0); t1 += dpp<ROR4>(t1);
  t0 += dpp<ROR8>(t0); t1 += dpp<ROR8>(t1);
  return rb(sum_rows_xy(t0, t1));
}

constexpr int L_RED = 0;
constexpr int L_RED2 = 16 * 192;
constexpr int L_Q = L_RED2 + 16 * 128;
constexpr int L_GU = L_Q + 96;
constexpr int L_WM = L_GU + 64;
constexpr int L_DT = L_WM + 16;
constexpr int L_END = 21504;

__global__ void __launch_bounds__(512, 2)
fused_decode(const u16* x_, const u16* kc_, const u16* vc_, const u16* wq_, const u16* wo_, const u16* wg_, const u16* wu_,
             const u16* wd_, u16* out_, unsigned char* ws_) {
  __shared__ __attribute__((aligned(16))) float lds[L_END];
  const int b = blockIdx.x, xg = b & 7, idx = b >> 3, h = 4 * xg + (idx & 3), s = idx >> 2;
  const int tid = threadIdx.x, lane = tid & 63, w = __builtin_amdgcn_readfirstlane(tid >> 6);
  const int u = lane >> 3, c8 = lane & 7, g = lane >> 4, n16 = lane & 15;
  const unsigned dt0 = clock_lo();
  v16u ka; v4u kb;
  asm volatile("s_load_dwordx16 %0, %2, 0x0\n\ts_load_dwordx4 %1, %2, 0x40" : "=&s"(ka), "=&s"(kb) : "s"(__builtin_amdgcn_kernarg_segment_ptr()));
  PIN();
  unsigned cpf0 = 0, cpf1 = 0;
  if (w == 0 && idx >= 30) {
    u64 pc;
    asm volatile("s_getpc_b64 %0" : "=s"(pc));
    const rsrc_t rc = mk((const void*)pc, CODEPF);
    asm volatile("buffer_load_dword %0, %2, %3, 0 offen\n\tbuffer_load_dword %1, %2, %3, %4 offen"
                 : "+v"(cpf0), "+v"(cpf1) : "v"(lane * 128), "s"(rc), "s"(8192));
  }
  PIN();
  const int rgl = idx / 5, cb = idx - 5 * rgl;
  const bool tile = idx < 30;
  int astride = rgl < 4 ? NH * DH * 2 : FF * 2;
  int aoff = 128 * cb * astride + (rgl < 4 ? 1536 * xg + 384 * rgl : 384 * xg);
  int abytes = tile ? 127 * astride + 384 : 0;
  int asel = rgl < 4 ? 0 : rgl - 3;
  int xro = (128 * cb + 16 * w + 2 * u) * 2;
  int ao = (16 * w + 2 * u) * astride + c8 * 16;
  unsigned kvoff = ((unsigned)h * SK + (SK - WIN)) * (DH * 2);
  int kvo = u * 384 + c8 * 16, kvs = w * 3072 + s * 24576;
  asm volatile("" : "+s"(astride), "+s"(aoff), "+s"(abytes), "+s"(asel), "+s"(kvoff), "+s"(kvs), "+v"(xro), "+v"(ao), "+v"(kvo));
  PIN();
  u64 ts64;
  asm volatile("s_waitcnt lgkmcnt(0)\n\ts_memrealtime %2" : "+s"(ka), "+s"(kb), "=&s"(ts64));
#define KARG(lo, hi) (((u64)(hi) << 32) | (lo))
  const u16 *x = (const u16*)KARG(ka[0], ka[1]), *kc = (const u16*)KARG(ka[2], ka[3]), *vc = (const u16*)KARG(ka[4], ka[5]);
  const u16 *wq = (const u16*)KARG(ka[6], ka[7]), *wo = (const u16*)KARG(ka[8], ka[9]), *wg = (const u16*)KARG(ka[10], ka[11]);
  const u16 *wu = (const u16*)KARG(ka[12], ka[13]), *wd = (const u16*)KARG(ka[14], ka[15]);
  u16* out = (u16*)KARG(kb[0], kb[1]);
  unsigned char* ws = (unsigned char*)KARG(kb[2], kb[3]);

  const rsrc_t ra = mk((const char*)(asel == 0 ? wq : (asel == 1 ? wg : wu)) + aoff, abytes);
  v4u a[6];
#pragma unroll
  for (int t = 0; t < 3; ++t) a[t] = ld16<AUX_NT>(ra, ao + t * 128);
  PIN();
  const rsrc_t rx = mk(x, D * 2);
  const v4u xs = ld16<0>(rx, lane * 16);
  const unsigned xs4 = __builtin_amdgcn_raw_buffer_load_b32(rx, lane * 4 + 1024, 0, 0);
  const unsigned xr = __builtin_amdgcn_raw_buffer_load_b32(rx, xro, 0, 0);
  PIN();
#pragma unroll
  for (int t = 0; t < 3; ++t) a[3 + t] = ld16<AUX_NT>(ra, ao + t * 128, astride);
  PIN();
  const rsrc_t rws = mk(ws, WS_END);
  const unsigned epoch = __builtin_amdgcn_raw_buffer_load_b32(rws, WS_EPOCH, 0, AUX_SC1);
  const rsrc_t rk = mk((const char*)kc + kvoff, WIN * DH * 2), rv = mk((const char*)vc + kvoff, WIN * DH * 2);
  lockstep();
  v4u kv[48];
#define ISSUE(n) do { _Pragma("unroll") for (int t_ = 0; t_ < 3; ++t_) { \
    kv[3 * (n) + t_] = ld16<AUX_NT>((n) < 8 ? rk : rv, kvo + t_ * 128, kvs + (((n) + h) & 7) * 196608); PIN(); } } while (0)
  ISSUE(0);
  v4u mask;
  {
    const unsigned hm = (lane & 1) ? 0xffff0000u : 0x0000ffffu;
    const int r = c8 >> 1;
    mask = v4u{r == 0 ? hm : 0u, r == 1 ? hm : 0u, r == 2 ? hm : 0u, r == 3 ? hm : 0u};
  }
  pace<10>();
  ISSUE(1);
  float xn0, xn1;
  {
    const unsigned xv[5] = {xs.x, xs.y, xs.z, xs.w, xs4};
    float sum = 0.f;
#pragma unroll
    for (int j = 0; j < 5; ++j) sum += bflo(xv[j]) + bfhi(xv[j]);
    const float mean = wave_sum(sum) * (1.f / D);
    float sq = 0.f;
#pragma unroll
    for (int j = 0; j < 5; ++j) { const float d0 = bflo(xv[j]) - mean, d1 = bfhi(xv[j]) - mean; sq += d0 * d0 + d1 * d1; }
    const float var = rb(wave_sum(sq) * (1.f / D));
    const float mb = rb(mean), sd = rb(sqrtf(rb(var + 1e-5f)));
    xn0 = rb(bflo(xr) - mb) / sd;
    xn1 = rb(bfhi(xr) - mb) / sd;
  }
  asm volatile("" : "+v"(xn0) : "v"(cpf0), "v"(cpf1));
  const bool valid = ((lane >> 3) & 1) == (lane >> 5);
  float* const redp = lds + L_RED + (w * 2 + (lane >> 5)) * 192 + 8 * c8 + 4 * (g & 1);
  {
    const v4u d0 = diag(xn0, mask), d1 = diag(xn1, mask);
    f32x4 acc[3];
#pragma unroll
    for (int t = 0; t < 3; ++t) acc[t] = mfma(d0, a[t], f32x4{0.f, 0.f, 0.f, 0.f});
#pragma unroll
    for (int t = 0; t < 3; ++t) acc[t] = mfma(d1, a[3 + t], acc[t]);
    if (valid) {
#pragma unroll
      for (int t = 0; t < 3; ++t) *(f32x4*)(redp + 64 * t) = acc[t];
    }
    PIN();
  }
  ISSUE(2);
  const unsigned tag = (unsigned)__builtin_amdgcn_readfirstlane((int)epoch) * 0x9E3779B1u + 0x7F4A7C15u;
  asm volatile("s_waitcnt lgkmcnt(0)" : "+s"(ts64) :: "memory");
  const unsigned ts = (unsigned)ts64 + EARLY;
#define SLOT0(n) not_before(ts + (((n) * TAU) >> 4))
  wg_barrier();
  const float* const sump = lds + L_RED + 4 * g * 192 + 32 * w + 2 * n16;
#define SUM192() sum_rows2((*(const f32x2*)sump + *(const f32x2*)(sump + 192)) + (*(const f32x2*)(sump + 384) + *(const f32x2*)(sump + 576)))
  unsigned tref;
  {
    f32x2 sum = SUM192();
    tref = clock_lo();
    if (w == 6) sum.x = asf(tref);
    publish(rws, !tile ? OOB : ((w < 6 && lane < 16) ? WS_QP + (xg * 30 + idx) * P_STRIDE + (16 * w + lane) * 16
                                                     : ((tid == 384 && cb == 2) ? WS_T0 + (rgl * 8 + xg) * 16 : OOB)), sum, tag);
  }
  PIN();
  const int yb = (cb * 48 + 6 * xg + rgl) * Y_STRIDE;
  const int ob = WS_OP + (h * 8 + s) * P_STRIDE;
  const rsrc_t rd = mk(wd + (size_t)(32 * (6 * xg + rgl)) * D + 128 * cb, tile ? 31 * D * 2 + 256 : 0);
  const rsrc_t ro = mk(wo + (size_t)(128 * (6 * xg + rgl)) * D + 128 * cb, tile ? 127 * D * 2 + 256 : 0);
  const bool glive = w < 6 || tile;
  const int gb = WS_QP + (xg * 30 + 5 * (w < 6 ? (idx & 3) : w - 2)) * P_STRIDE + (16 * (w < 6 ? w : rgl) + n16) * 16;
  const int go0 = glive ? gb + g * P_STRIDE : OOB;
  const int wl = 48 * w + lane - 16;
  const int go1 = g == 0 ? (glive ? gb + 4 * P_STRIDE : OOB)
                         : (wl < 96 ? ob + wl * 16
                                    : (wl < 224 ? (tile ? (wl < 160 ? WS_YP - 96 * 16 : WS_ZP - 160 * 16) + yb + wl * 16 : OOB)
                                                : (w == 7 ? WS_T0 + (lane - 16) * 16 : OOB)));
  pace<7>(); SLOT0(3); ISSUE(3); lockstep();
  pace<7>(); SLOT0(4); ISSUE(4); lockstep();
  pace<7>(); SLOT0(5);
  v4u g0 = ld16<AUX_SC1>(rws, go0), g1 = ld16<AUX_SC1>(rws, go1);
  lockstep();
  ISSUE(5); lockstep();
  pace<8>(); SLOT0(6); ISSUE(6); lockstep();
  pace<6>(); SLOT0(7); ISSUE(7);
#define GATHER_OK() (!glive || (missing(g0, tag) | (missing(g1, tag) & 0xffff)) == 0)
  if (__builtin_expect(!GATHER_OK(), 0)) {
    unsigned spins = 0;
#pragma unroll 1
    do {
      __builtin_amdgcn_s_sleep(2);
      reload2(g0, g1, rws, go0, go1);
    } while (!GATHER_OK() && ++spins < SPIN_CAP);
  }
  float* const red2p = lds + L_RED2 + w * 128 + 8 * n16 + 4 * (g & 1);
  {
    const f32x2 q = sum_rows2(f32x2{asf(g0.x), asf(g0.z)}) + f32x2{asf(g1.x), asf(g1.z)};
    if (lane < 16) {
      if (w < 6) ((unsigned*)(lds + L_Q))[w * 16 + lane] = pk(q.x, q.y);
      else *(f32x2*)(lds + L_GU + (w - 6) * 32 + 2 * lane) = q;
    }
    if (w == 7) {
      const bool have = g != 0 && tagged(g1, tag);
      int e = have ? (int)(tref - g1.x) : 0;
      e = e < -400 ? -400 : (e > 400 ? 400 : e);
      const float cnt = wave_sum(have ? 1.f : 0.f), em = wave_sum((float)e) * __builtin_amdgcn_rcpf(fmaxf(cnt, 1.f));
      if (lane == 0) lds[L_DT] = em;
    }
  }
  wg_barrier();
  float sc[8];
  v4u qk[3];
#pragma unroll
  for (int t = 0; t < 3; ++t) qk[t] = *(const v4u*)(lds + L_Q + 32 * t + 4 * c8);
  tref += LEAD - (unsigned)__builtin_amdgcn_readfirstlane((int)lds[L_DT]);
  float wmax = -INFINITY;
#define QK(i) do { float d_[3]; \
    _Pragma("unroll") for (int t_ = 0; t_ < 3; ++t_) { d_[t_] = 0.f; _Pragma("unroll") for (int c_ = 0; c_ < 4; ++c_) d_[t_] = dot2(kv[3 * (i) + t_][c_], qk[t_][c_], d_[t_]); } \
    sc[i] = rb(rb(sum8((d_[0] + d_[1]) + d_[2])) * 0.07216878364870322f); wmax = fmaxf(wmax, sc[i]); PIN(); } while (0)
#define SLOT(n) not_before(tref + (((n) * TAU) >> 4))
  QK(0);
  pace<6>(); SLOT(8); ISSUE(8);
  QK(1); QK(2); QK(3);
  lockstep();
  pace<6>(); SLOT(9); ISSUE(9);
  QK(4); QK(5); QK(6);
  lockstep();
  pace<6>(); SLOT(10); ISSUE(10);
  QK(7);
  constexpr float LOG2E = 1.4426950408889634f;
  {
    wmax = max_u(wmax);
    float lsum = 0.f;
#pragma unroll
    for (int i = 0; i < 8; ++i) lsum += __builtin_amdgcn_exp2f((sc[i] - wmax) * LOG2E);
    lsum = sum_u(lsum);
    if (lane == 0) *(f32x2*)(lds + L_WM + 2 * w) = f32x2{wmax, lsum};
  }
  wg_barrier();
  {
    const f32x2 wm = *(const f32x2*)(lds + L_WM + 2 * c8);
    const float M = max8(wm.x);
    publish(rws, tid == 384 ? WS_ML + (h * 8 + s) * 16 : OOB, f32x2{M, sum8(__builtin_amdgcn_exp2f((wm.x - M) * LOG2E) * wm.y)}, tag);
    PIN();
  }
  pace<7>(); SLOT(11); ISSUE(11);
  lockstep();
  pace<7>(); SLOT(12); ISSUE(12);
  unsigned hp;
  {
    const float ga = lds[L_GU + 4 * w + g], up = lds[L_GU + 32 + 4 * w + g];
    const float hv = rb(gelu_tanh(rb(ga))) * rb(up);
    hp = pk(hv, hv);
    asm volatile("" : "+v"(hp));
  }
  lockstep();
  pace<7>(); SLOT(13); ISSUE(13); lockstep();
  pace<6>(); SLOT(14);
  const int mo = WS_ML + (h * 8 + c8) * 16;
  v4u ml = ld16<AUX_SC1>(rws, mo);
  PIN();
  ISSUE(14); lockstep();
  pace<7>(); SLOT(15); ISSUE(15);
  const int gd = 128 * rgl + 16 * w, hl = (gd * 683) >> 17;
  int o0 = WS_OP + ((4 * xg + hl) * 8 + u) * P_STRIDE + ((gd - 192 * hl) / 2 + c8) * 16;
  int ost = lane < 16 ? ob + (16 * w + lane) * 16 : OOB, yst = WS_YP + yb + lane * 16;
  asm volatile("" : "+v"(o0), "+v"(ost), "+v"(yst));
  lockstep();
  if (__builtin_expect(missing(ml, tag) != 0, 0)) {
    unsigned spins = 0;
#pragma unroll 1
    do { reload1(ml, rws, mo); } while (missing(ml, tag) != 0 && ++spins < SPIN_CAP);
  }
  {
    const float ms = asf(ml.x), M = max8(ms);
    const float inv = 1.f / sum8(__builtin_amdgcn_exp2f((ms - M) * LOG2E) * asf(ml.z));
#pragma unroll
    for (int i = 0; i < 8; ++i) sc[i] = __builtin_amdgcn_exp2f((sc[i] - M) * LOG2E) * inv;
  }
  f32x4 acc[3] = {f32x4{0.f, 0.f, 0.f, 0.f}, f32x4{0.f, 0.f, 0.f, 0.f}, f32x4{0.f, 0.f, 0.f, 0.f}};
#define PV(i) do { const v4u dp_ = diag(sc[i], mask); \
    _Pragma("unroll") for (int t_ = 0; t_ < 3; ++t_) acc[t_] = mfma(dp_, kv[24 + 3 * (i) + t_], acc[t_]); PIN(); } while (0)
#pragma unroll
  for (int i = 0; i < 4; ++i) PV(i);
  SLOT(16);
  const v4u wdr = ld16<AUX_NT>(rd, (4 * w + g) * 1280 + n16 * 16);
  PIN();
  v4u wor[4];
#pragma unroll
  for (int r = 0; r < 4; ++r) wor[r] = ld16<AUX_NT>(ro, (16 * w + 8 * (g >> 1) + (g & 1)) * 1280 + n16 * 16, r * 2560);
  PIN();
#pragma unroll
  for (int i = 4; i < 8; ++i) PV(i);
  if (valid) {
#pragma unroll
    for (int t = 0; t < 3; ++t) *(f32x4*)(redp + 64 * t) = acc[t];
  }
  wg_barrier();
  if (w < 6) publish(rws, ost, SUM192(), tag);
  const float* const sum2p = lds + L_RED2 + (lane >> 5) * 512 + 4 * (lane & 31);
#define SUM128(o) ({ const f32x4 s_ = (*(const f32x4*)(sum2p + (o)) + *(const f32x4*)(sum2p + (o) + 128)) + (*(const f32x4*)(sum2p + (o) + 256) + *(const f32x4*)(sum2p + (o) + 384)); \
    const auto a_ = __builtin_amdgcn_permlane32_swap(asu(s_[0]), asu(s_[2]), false, false), b_ = __builtin_amdgcn_permlane32_swap(asu(s_[1]), asu(s_[3]), false, false); \
    f32x2{asf(a_[0]), asf(b_[0])} + f32x2{asf(a_[1]), asf(b_[1])}; })
  unsigned spins = 0;
  if (tile) {
    const f32x4 z = mfma(v4u{hp, hp, hp, hp} & mask, wdr, f32x4{0.f, 0.f, 0.f, 0.f});
    if (lane < 32) *(f32x4*)(red2p + 1024) = z;
    wg_barrier();
    if (w == 7) publish(rws, yst + (WS_ZP - WS_YP), SUM128(1024), tag);
    if (w != 6) pace<1>(); else pace<0>();
    v4u e0;
#pragma unroll 1
    for (;;) {
      reload1(e0, rws, o0);
      if (missing(e0, tag) == 0) break;
      if (++spins >= SPIN_CAP) break;
    }
    float t0 = asf(e0.x), t1 = asf(e0.z);
    t0 += dpp<ROR8>(t0); t1 += dpp<ROR8>(t1);
    const float ov = sum_rows_xy(t0, t1);
    unsigned op = pk(ov, ov);
    op = (unsigned)__builtin_amdgcn_update_dpp((int)op, (int)op, ROR4, 0xc, 0xf, false);
    f32x4 y = mfma(diag_lane<0>(op, mask), wor[0], f32x4{0.f, 0.f, 0.f, 0.f});
    y = mfma(diag_lane<1>(op, mask), wor[1], y);
    y = mfma(diag_lane<2>(op, mask), wor[2], y);
    y = mfma(diag_lane<3>(op, mask), wor[3], y);
    if (lane < 32) *(f32x4*)red2p = y;
    wg_barrier();
    if (w == 0) {
      publish(rws, yst, SUM128(0), tag);
    }
  } else if (w >= 3) {
    const int unit = 5 * (xg + 8 * (idx - 30)) + w - 3;
    const int slot = 4 * (unit & 15) + (lane & 3);
    const int col = 128 * (unit >> 4) + 4 * (slot & 31) + 2 * (slot >> 5) + (g & 1);
    const int fo = ((unit >> 4) * 48 + (lane >> 2)) * Y_STRIDE + slot * 16;
    float xo = bflo(__builtin_amdgcn_raw_buffer_load_b16(rx, col * 2, 0, 0));
    v4u f0, f1, f2;
#pragma unroll 1
    do {
      reload3(f0, f1, f2, rws, WS_ZP + fo, WS_ZP + fo + 16 * Y_STRIDE, WS_ZP + fo + 32 * Y_STRIDE);
    } while ((missing(f0, tag) | missing(f1, tag) | missing(f2, tag)) != 0 && ++spins < SPIN_CAP);
    const float zz = colsum3(f0, f1, f2);
    float gt = rb(zz / (1.f + __builtin_amdgcn_exp2f(-zz * LOG2E)));
    int oc = (lane < 32 && n16 < 4) ? col * 2 : OOB;
    int y0 = WS_YP + fo, y1 = y0 + 16 * Y_STRIDE, y2 = y0 + 32 * Y_STRIDE;
    asm volatile("" : "+v"(gt), "+v"(xo), "+v"(oc), "+v"(y0), "+v"(y1), "+v"(y2));
    const rsrc_t rout = mk(out, D * 2);
#pragma unroll 1
    for (;;) {
      reload3(f0, f1, f2, rws, y0, y1, y2);
      if ((missing(f0, tag) | missing(f1, tag) | missing(f2, tag)) == 0) break;
      if (++spins >= SPIN_CAP) break;
    }
    __builtin_amdgcn_raw_buffer_store_b16((u16)pk(xo + rb(colsum3(f0, f1, f2) * gt), 0.f), rout, oc, 0, AUX_SC1);
    if (unit == 0) __builtin_amdgcn_raw_buffer_store_b32(tag, rws, lane == 0 ? WS_EPOCH : OOB, 0, AUX_SC1);
    { const unsigned te = clock_lo(); not_before(te + 9 * (te - dt0)); }
  }
}

extern "C" void kernel_launch(void* const* d_in, const int* in_sizes, int n_in, void* d_out, int out_size, void* d_ws, size_t ws_size,
                              hipStream_t stream) {
  fused_decode<<<256, 512, 0, stream>>>((const u16*)d_in[0], (const u16*)d_in[1], (const u16*)d_in[2], (const u16*)d_in[3],
                                        (const u16*)d_in[4], (const u16*)d_in[6], (const u16*)d_in[7], (const u16*)d_in[8],
                                        (u16*)d_out, (unsigned char*)d_ws);
}
```

```cpp
#include <hip/hip_runtime.h>
#include <hip/hip_bf16.h>

typedef unsigned short u16;
typedef unsigned v4u __attribute__((ext_vector_type(4)));
typedef unsigned v16u __attribute__((ext_vector_type(16)));
typedef float f32x4 __attribute__((ext_vector_type(4)));
typedef float f32x2 __attribute__((ext_vector_type(2)));
typedef __bf16 bf16x8 __attribute__((ext_vector_type(8)));
typedef __bf16 bf16x2 __attribute__((ext_vector_type(2)));
typedef __amdgpu_buffer_rsrc_t rsrc_t;
typedef unsigned long long u64;

constexpr int D = 640, NH = 32, DH = 192, SK = 32768, WIN = 4096, FF = 1536;
constexpr int AUX_NT = 2, AUX_SC1 = 16;
constexpr int OOB = 0x40000000;
constexpr unsigned SPIN_CAP = 1u << 16;

constexpr int WS_EPOCH = 0;
constexpr int P_STRIDE = 1664, Y_STRIDE = 1152;
constexpr int WS_T0 = 2048;
constexpr int WS_ML = 4096;
constexpr int WS_QP = 8192;
constexpr int WS_OP = WS_QP + 240 * P_STRIDE;
constexpr int WS_ZP = WS_OP + 256 * P_STRIDE;
constexpr int WS_YP = WS_ZP + 240 * Y_STRIDE;
constexpr int WS_END = WS_YP + 240 * Y_STRIDE;

#define TAU 1376
#define EARLY 150
#define LEAD -158
#define CODEPF 9300

__device__ __forceinline__ rsrc_t mk(const void* p, int bytes) { return __builtin_amdgcn_make_buffer_rsrc((void*)p, 0, bytes, 0x00020000); }
template <int AUX> __device__ __forceinline__ v4u ld16(rsrc_t r, int voff, int soff = 0) { return __builtin_amdgcn_raw_buffer_load_b128(r, voff, soff, AUX); }
__device__ __forceinline__ float asf(unsigned u) { return __builtin_bit_cast(float, u); }
__device__ __forceinline__ unsigned asu(float f) { return __builtin_bit_cast(unsigned, f); }
__device__ __forceinline__ void publish(rsrc_t r, int voff, f32x2 v, unsigned tag) {
  __builtin_amdgcn_raw_buffer_store_b128(v4u{asu(v.x), tag, asu(v.y), tag}, r, voff, 0, AUX_SC1);
}
__device__ __forceinline__ float bflo(unsigned u) { return asf(u << 16); }
__device__ __forceinline__ float bfhi(unsigned u) { return asf(u & 0xffff0000u); }
__device__ __forceinline__ unsigned pk(float lo, float hi) {
  bf16x2 r = {(__bf16)lo, (__bf16)hi};
  return __builtin_bit_cast(unsigned, r);
}
__device__ __forceinline__ float rb(float f) { return (float)(__bf16)f; }
__device__ __forceinline__ float dot2(unsigned a, unsigned b, float c) {
  return __builtin_amdgcn_fdot2_f32_bf16(__builtin_bit_cast(bf16x2, a), __builtin_bit_cast(bf16x2, b), c, false);
}
template <int CTRL> __device__ __forceinline__ float dpp(float x) {
  return asf((unsigned)__builtin_amdgcn_mov_dpp((int)asu(x), CTRL, 0xf, 0xf, true));
}
constexpr int XOR1 = 0xB1, XOR2 = 0x4E, HMIR = 0x141, ROR8 = 0x128, ROR4 = 0x124, BCAST = 0x150;
__device__ __forceinline__ float sum8(float x) { x += dpp<XOR1>(x); x += dpp<XOR2>(x); x += dpp<HMIR>(x); return x; }
__device__ __forceinline__ float max8(float x) { x = fmaxf(x, dpp<XOR1>(x)); x = fmaxf(x, dpp<XOR2>(x)); x = fmaxf(x, dpp<HMIR>(x)); return x; }
__device__ __forceinline__ float sum_rows(float x) {
  auto s = __builtin_amdgcn_permlane16_swap(asu(x), asu(x), false, false);
  x = asf(s[0]) + asf(s[1]);
  auto t = __builtin_amdgcn_permlane32_swap(asu(x), asu(x), false, false);
  return asf(t[0]) + asf(t[1]);
}
__device__ __forceinline__ float max_rows(float x) {
  auto s = __builtin_amdgcn_permlane16_swap(asu(x), asu(x), false, false);
  x = fmaxf(asf(s[0]), asf(s[1]));
  auto t = __builtin_amdgcn_permlane32_swap(asu(x), asu(x), false, false);
  return fmaxf(asf(t[0]), asf(t[1]));
}
__device__ __forceinline__ float sum_u(float x) { x += dpp<ROR8>(x); return sum_rows(x); }
__device__ __forceinline__ float max_u(float x) { x = fmaxf(x, dpp<ROR8>(x)); return max_rows(x); }
__device__ __forceinline__ float wave_sum(float x) { return sum_u(sum8(x)); }
__device__ __forceinline__ float sum_rows_xy(float x, float y) {
  auto s = __builtin_amdgcn_permlane16_swap(asu(x), asu(y), false, false);
  const float t = asf(s[0]) + asf(s[1]);
  auto r = __builtin_amdgcn_permlane32_swap(asu(t), asu(t), false, false);
  return asf(r[0]) + asf(r[1]);
}
__device__ __forceinline__ f32x2 sum_rows2(f32x2 v) {
  auto a = __builtin_amdgcn_permlane16_swap(asu(v.x), asu(v.x), false, false), b = __builtin_amdgcn_permlane16_swap(asu(v.y), asu(v.y), false, false);
  v = f32x2{asf(a[0]), asf(b[0])} + f32x2{asf(a[1]), asf(b[1])};
  auto c = __builtin_amdgcn_permlane32_swap(asu(v.x), asu(v.x), false, false), d = __builtin_amdgcn_permlane32_swap(asu(v.y), asu(v.y), false, false);
  return f32x2{asf(c[0]), asf(d[0])} + f32x2{asf(c[1]), asf(d[1])};
}
__device__ __forceinline__ v4u diag(float val, const v4u& mask) { const unsigned p = pk(val, val); return v4u{p, p, p, p} & mask; }
template <int L> __device__ __forceinline__ v4u diag_lane(unsigned p, const v4u& mask) {
  const unsigned d = (unsigned)__builtin_amdgcn_mov_dpp((int)p, BCAST + L, 0xf, 0xf, true);
  return v4u{d, d, d, d} & mask;
}
__device__ __forceinline__ f32x4 mfma(v4u a, v4u b, f32x4 c) {
  return __builtin_amdgcn_mfma_f32_16x16x32_bf16(__builtin_bit_cast(bf16x8, a), __builtin_bit_cast(bf16x8, b), c, 0, 0, 0);
}
#define PIN() __builtin_amdgcn_sched_barrier(0)
__device__ __forceinline__ void wg_barrier() {
  asm volatile("s_waitcnt lgkmcnt(0)" ::: "memory");
  __builtin_amdgcn_s_barrier();
  asm volatile("" ::: "memory");
}
__device__ __forceinline__ void lockstep() { PIN(); __builtin_amdgcn_s_barrier(); PIN(); }
template <int N> __device__ __forceinline__ void pace() { PIN(); asm volatile("s_waitcnt vmcnt(%0)" :: "i"(N)); PIN(); }
__device__ __forceinline__ unsigned clock_lo() {
  u64 t;
  asm volatile("s_memrealtime %0\n\ts_waitcnt lgkmcnt(0)" : "=s"(t) :: "memory");
  return (unsigned)t;
}
__device__ __forceinline__ void not_before(unsigned t) {
  PIN();
  asm volatile("1:\n\ts_memrealtime vcc\n\ts_waitcnt lgkmcnt(0)\n\ts_sub_u32 vcc_lo, vcc_lo, %0\n\ts_cmp_lt_i32 vcc_lo, 0\n\ts_cbranch_scc1 1b"
               :: "s"(t) : "vcc", "scc", "memory");
  PIN();
}
__device__ __forceinline__ bool tagged(const v4u& g, unsigned tag) { return (g.y == tag) & (g.w == tag); }
__device__ __forceinline__ u64 missing(const v4u& g, unsigned tag) { return __builtin_amdgcn_ballot_w64(g.y != tag) | __builtin_amdgcn_ballot_w64(g.w != tag); }
__device__ __forceinline__ void reload1(v4u& a, rsrc_t rs, int oa) {
  asm volatile("buffer_load_dwordx4 %0, %1, %2, 0 offen sc1\n\ts_waitcnt vmcnt(0)" : "=&v"(a) : "v"(oa), "s"(rs) : "memory");
}
__device__ __forceinline__ void reload2(v4u& a, v4u& b, rsrc_t rs, int oa, int ob) {
  asm volatile("buffer_load_dwordx4 %0, %2, %4, 0 offen sc1\n\tbuffer_load_dwordx4 %1, %3, %4, 0 offen sc1\n\ts_waitcnt vmcnt(0)"
               : "=&v"(a), "=&v"(b) : "v"(oa), "v"(ob), "s"(rs) : "memory");
}
__device__ __forceinline__ void reload3(v4u& a, v4u& b, v4u& c, rsrc_t rs, int oa, int ob, int oc) {
  asm volatile("buffer_load_dwordx4 %0, %3, %6, 0 offen sc1\n\tbuffer_load_dwordx4 %1, %4, %6, 0 offen sc1\n\t"
               "buffer_load_dwordx4 %2, %5, %6, 0 offen sc1\n\ts_waitcnt vmcnt(0)"
               : "=&v"(a), "=&v"(b), "=&v"(c) : "v"(oa), "v"(ob), "v"(oc), "s"(rs) : "memory");
}
__device__ __forceinline__ float gelu_tanh(float x) {
  const float u = 0.7978845608028654f * (x + 0.044715f * x * x * x);
  const float e = __builtin_amdgcn_exp2f(u * 2.885390081777927f);
  const float th = 1.f - 2.f / (e + 1.f);
  return 0.5f * x * (1.f + th);
}
__device__ __forceinline__ float colsum3(const v4u& a, const v4u& b, const v4u& c) {
  float t0 = (asf(a.x) + asf(b.x)) + asf(c.x), t1 = (asf(a.z) + asf(b.z)) + asf(c.z);
  t0 += dpp<ROR4>(t0); t1 += dpp<ROR4>(t1);
  t0 += dpp<ROR8>(t0); t1 += dpp<ROR8>(t1);
  return rb(sum_rows_xy(t0, t1));
}

constexpr int L_RED = 0;
constexpr int L_RED2 = 16 * 192;
constexpr int L_Q = L_RED2 + 16 * 128;
constexpr int L_GU = L_Q + 96;
constexpr int L_WM = L_GU + 64;
constexpr int L_DT = L_WM + 16;
constexpr int L_END = 21504;

__global__ void __launch_bounds__(512, 2)
fused_decode(const u16* x_, const u16* kc_, const u16* vc_, const u16* wq_, const u16* wo_, const u16* wg_, const u16* wu_,
             const u16* wd_, u16* out_, unsigned char* ws_) {
  __shared__ __attribute__((aligned(16))) float lds[L_END];
  const int b = blockIdx.x, xg = b & 7, idx = b >> 3, h = 4 * xg + (idx & 3), s = idx >> 2;
  const int tid = threadIdx.x, lane = tid & 63, w = __builtin_amdgcn_readfirstlane(tid >> 6);
  const int u = lane >> 3, c8 = lane & 7, g = lane >> 4, n16 = lane & 15;
  v16u ka; v4u kb; u64 ts64;
  asm volatile("s_load_dwordx16 %0, %3, 0x0\n\ts_load_dwordx4 %1, %3, 0x40\n\ts_memrealtime %2"
               : "=&s"(ka), "=&s"(kb), "=&s"(ts64) : "s"(__builtin_amdgcn_kernarg_segment_ptr()));
  PIN();
  unsigned cpf0 = 0, cpf1 = 0;
  if (w == 0 && idx >= 30) {
    u64 pc;
    asm volatile("s_getpc_b64 %0" : "=s"(pc));
    const rsrc_t rc = mk((const void*)pc, CODEPF);
    asm volatile("buffer_load_dword %0, %2, %3, 0 offen\n\tbuffer_load_dword %1, %2, %3, %4 offen"
                 : "+v"(cpf0), "+v"(cpf1) : "v"(lane * 128), "s"(rc), "s"(8192));
  }
  PIN();
  const int rgl = idx / 5, cb = idx - 5 * rgl;
  const bool tile = idx < 30;
  int astride = rgl < 4 ? NH * DH * 2 : FF * 2;
  int aoff = 128 * cb * astride + (rgl < 4 ? 1536 * xg + 384 * rgl : 384 * xg);
  int abytes = tile ? 127 * astride + 384 : 0;
  int asel = rgl < 4 ? 0 : rgl - 3;
  int xro = (128 * cb + 16 * w + 2 * u) * 2;
  int ao = (16 * w + 2 * u) * astride + c8 * 16;
  unsigned kvoff = ((unsigned)h * SK + (SK - WIN)) * (DH * 2);
  int kvo = u * 384 + c8 * 16, kvs = w * 3072 + s * 24576;
  asm volatile("" : "+s"(astride), "+s"(aoff), "+s"(abytes), "+s"(asel), "+s"(kvoff), "+s"(kvs), "+v"(xro), "+v"(ao), "+v"(kvo));
  PIN();
  asm volatile("s_waitcnt lgkmcnt(0)" : "+s"(ka), "+s"(kb), "+s"(ts64));
  const unsigned ts = (unsigned)ts64 + EARLY;
#define SLOT0(n) not_before(ts + (((n) * TAU) >> 4))
#define KARG(lo, hi) (((u64)(hi) << 32) | (lo))
  const u16 *x = (const u16*)KARG(ka[0], ka[1]), *kc = (const u16*)KARG(ka[2], ka[3]), *vc = (const u16*)KARG(ka[4], ka[5]);
  const u16 *wq = (const u16*)KARG(ka[6], ka[7]), *wo = (const u16*)KARG(ka[8], ka[9]), *wg = (const u16*)KARG(ka[10], ka[11]);
  const u16 *wu = (const u16*)KARG(ka[12], ka[13]), *wd = (const u16*)KARG(ka[14], ka[15]);
  u16* out = (u16*)KARG(kb[0], kb[1]);
  unsigned char* ws = (unsigned char*)KARG(kb[2], kb[3]);

  const rsrc_t ra = mk((const char*)(asel == 0 ? wq : (asel == 1 ? wg : wu)) + aoff, abytes);
  v4u a[6];
#pragma unroll
  for (int t = 0; t < 3; ++t) a[t] = ld16<AUX_NT>(ra, ao + t * 128);
  PIN();
  const rsrc_t rx = mk(x, D * 2);
  const v4u xs = ld16<0>(rx, lane * 16);
  const unsigned xs4 = __builtin_amdgcn_raw_buffer_load_b32(rx, lane * 4 + 1024, 0, 0);
  const unsigned xr = __builtin_amdgcn_raw_buffer_load_b32(rx, xro, 0, 0);
  PIN();
#pragma unroll
  for (int t = 0; t < 3; ++t) a[3 + t] = ld16<AUX_NT>(ra, ao + t * 128, astride);
  PIN();
  const rsrc_t rws = mk(ws, WS_END);
  const unsigned epoch = __builtin_amdgcn_raw_buffer_load_b32(rws, WS_EPOCH, 0, AUX_SC1);
  const rsrc_t rk = mk((const char*)kc + kvoff, WIN * DH * 2), rv = mk((const char*)vc + kvoff, WIN * DH * 2);
  lockstep();
  v4u kv[48];
#define ISSUE(n) do { _Pragma("unroll") for (int t_ = 0; t_ < 3; ++t_) { \
    kv[3 * (n) + t_] = ld16<AUX_NT>((n) < 8 ? rk : rv, kvo + t_ * 128, kvs + (((n) + h) & 7) * 196608); PIN(); } } while (0)
  ISSUE(0);
  v4u mask;
  {
    const unsigned hm = (lane & 1) ? 0xffff0000u : 0x0000ffffu;
    const int r = c8 >> 1;
    mask = v4u{r == 0 ? hm : 0u, r == 1 ? hm : 0u, r == 2 ? hm : 0u, r == 3 ? hm : 0u};
  }
  pace<10>();
  ISSUE(1);
  float xn0, xn1;
  {
    const unsigned xv[5] = {xs.x, xs.y, xs.z, xs.w, xs4};
    float sum = 0.f;
#pragma unroll
    for (int j = 0; j < 5; ++j) sum += bflo(xv[j]) + bfhi(xv[j]);
    const float mean = wave_sum(sum) * (1.f / D);
    float sq = 0.f;
#pragma unroll
    for (int j = 0; j < 5; ++j) { const float d0 = bflo(xv[j]) - mean, d1 = bfhi(xv[j]) - mean; sq += d0 * d0 + d1 * d1; }
    const float var = rb(wave_sum(sq) * (1.f / D));
    const float mb = rb(mean), sd = rb(sqrtf(rb(var + 1e-5f)));
    xn0 = rb(bflo(xr) - mb) / sd;
    xn1 = rb(bfhi(xr) - mb) / sd;
  }
  asm volatile("" : "+v"(xn0) : "v"(cpf0), "v"(cpf1));
  const bool valid = ((lane >> 3) & 1) == (lane >> 5);
  float* const redp = lds + L_RED + (w * 2 + (lane >> 5)) * 192 + 8 * c8 + 4 * (g & 1);
  {
    const v4u d0 = diag(xn0, mask), d1 = diag(xn1, mask);
    f32x4 acc[3];
#pragma unroll
    for (int t = 0; t < 3; ++t) acc[t] = mfma(d0, a[t], f32x4{0.f, 0.f, 0.f, 0.f});
#pragma unroll
    for (int t = 0; t < 3; ++t) acc[t] = mfma(d1, a[3 + t], acc[t]);
    if (valid) {
#pragma unroll
      for (int t = 0; t < 3; ++t) *(f32x4*)(redp + 64 * t) = acc[t];
    }
    PIN();
  }
  ISSUE(2);
  const unsigned tag = (unsigned)__builtin_amdgcn_readfirstlane((int)epoch) * 0x9E3779B1u + 0x7F4A7C15u;
  wg_barrier();
  const float* const sump = lds + L_RED + 4 * g * 192 + 32 * w + 2 * n16;
#define SUM192() sum_rows2((*(const f32x2*)sump + *(const f32x2*)(sump + 192)) + (*(const f32x2*)(sump + 384) + *(const f32x2*)(sump + 576)))
  unsigned tref;
  {
    f32x2 sum = SUM192();
    tref = clock_lo();
    if (w == 6) sum.x = asf(tref);
    publish(rws, !tile ? OOB : ((w < 6 && lane < 16) ? WS_QP + (xg * 30 + idx) * P_STRIDE + (16 * w + lane) * 16
                                                     : ((tid == 384 && cb == 2) ? WS_T0 + (rgl * 8 + xg) * 16 : OOB)), sum, tag);
  }
  PIN();
  const int yb = (cb * 48 + 6 * xg + rgl) * Y_STRIDE;
  const int ob = WS_OP + (h * 8 + s) * P_STRIDE;
  const rsrc_t rd = mk(wd + (size_t)(32 * (6 * xg + rgl)) * D + 128 * cb, tile ? 31 * D * 2 + 256 : 0);
  const rsrc_t ro = mk(wo + (size_t)(128 * (6 * xg + rgl)) * D + 128 * cb, tile ? 127 * D * 2 + 256 : 0);
  const bool glive = w < 6 || tile;
  const int gb = WS_QP + (xg * 30 + 5 * (w < 6 ? (idx & 3) : w - 2)) * P_STRIDE + (16 * (w < 6 ? w : rgl) + n16) * 16;
  const int go0 = glive ? gb + g * P_STRIDE : OOB;
  const int wl = 48 * w + lane - 16;
  const int go1 = g == 0 ? (glive ? gb + 4 * P_STRIDE : OOB)
                         : (wl < 96 ? ob + wl * 16
                                    : (wl < 224 ? (tile ? (wl < 160 ? WS_YP - 96 * 16 : WS_ZP - 160 * 16) + yb + wl * 16 : OOB)
                                                : (w == 7 ? WS_T0 + (lane - 16) * 16 : OOB)));
  pace<7>(); SLOT0(3); ISSUE(3); lockstep();
  pace<7>(); SLOT0(4); ISSUE(4); lockstep();
  pace<7>(); SLOT0(5);
  v4u g0 = ld16<AUX_SC1>(rws, go0), g1 = ld16<AUX_SC1>(rws, go1);
  lockstep();
  ISSUE(5); lockstep();
  pace<8>(); SLOT0(6); ISSUE(6); lockstep();
  pace<6>(); SLOT0(7); ISSUE(7);
#define GATHER_OK() (!glive || (missing(g0, tag) | (missing(g1, tag) & 0xffff)) == 0)
  if (__builtin_expect(!GATHER_OK(), 0)) {
    unsigned spins = 0;
#pragma unroll 1
    do {
      __builtin_amdgcn_s_sleep(2);
      reload2(g0, g1, rws, go0, go1);
    } while (!GATHER_OK() && ++spins < SPIN_CAP);
  }
  float* const red2p = lds + L_RED2 + w * 128 + 8 * n16 + 4 * (g & 1);
  {
    const f32x2 q = sum_rows2(f32x2{asf(g0.x), asf(g0.z)}) + f32x2{asf(g1.x), asf(g1.z)};
    if (lane < 16) {
      if (w < 6) ((unsigned*)(lds + L_Q))[w * 16 + lane] = pk(q.x, q.y);
      else *(f32x2*)(lds + L_GU + (w - 6) * 32 + 2 * lane) = q;
    }
    if (w == 7) {
      const bool have = g != 0 && tagged(g1, tag);
      int e = have ? (int)(tref - g1.x) : 0;
      e = e < -400 ? -400 : (e > 400 ? 400 : e);
      const float cnt = wave_sum(have ? 1.f : 0.f), em = wave_sum((float)e) * __builtin_amdgcn_rcpf(fmaxf(cnt, 1.f));
      if (lane == 0) lds[L_DT] = em;
    }
  }
  wg_barrier();
  float sc[8];
  v4u qk[3];
#pragma unroll
  for (int t = 0; t < 3; ++t) qk[t] = *(const v4u*)(lds + L_Q + 32 * t + 4 * c8);
  tref += LEAD - (unsigned)__builtin_amdgcn_readfirstlane((int)lds[L_DT]);
  float wmax = -INFINITY;
#define QK(i) do { float d_[3]; \
    _Pragma("unroll") for (int t_ = 0; t_ < 3; ++t_) { d_[t_] = 0.f; _Pragma("unroll") for (int c_ = 0; c_ < 4; ++c_) d_[t_] = dot2(kv[3 * (i) + t_][c_], qk[t_][c_], d_[t_]); } \
    sc[i] = rb(rb(sum8((d_[0] + d_[1]) + d_[2])) * 0.07216878364870322f); wmax = fmaxf(wmax, sc[i]); PIN(); } while (0)
#define SLOT(n) not_before(tref + (((n) * TAU) >> 4))
  QK(0);
  pace<6>(); SLOT(8); ISSUE(8);
  QK(1); QK(2); QK(3);
  lockstep();
  pace<6>(); SLOT(9); ISSUE(9);
  QK(4); QK(5); QK(6);
  lockstep();
  pace<6>(); SLOT(10); ISSUE(10);
  QK(7);
  constexpr float LOG2E = 1.4426950408889634f;
  {
    wmax = max_u(wmax);
    float lsum = 0.f;
#pragma unroll
    for (int i = 0; i < 8; ++i) lsum += __builtin_amdgcn_exp2f((sc[i] - wmax) * LOG2E);
    lsum = sum_u(lsum);
    if (lane == 0) *(f32x2*)(lds + L_WM + 2 * w) = f32x2{wmax, lsum};
  }
  wg_barrier();
  {
    const f32x2 wm = *(const f32x2*)(lds + L_WM + 2 * c8);
    const float M = max8(wm.x);
    publish(rws, tid == 384 ? WS_ML + (h * 8 + s) * 16 : OOB, f32x2{M, sum8(__builtin_amdgcn_exp2f((wm.x - M) * LOG2E) * wm.y)}, tag);
    PIN();
  }
  pace<7>(); SLOT(11); ISSUE(11); lockstep();
  pace<7>(); SLOT(12); ISSUE(12);
  unsigned hp;
  {
    const float ga = lds[L_GU + 4 * w + g], up = lds[L_GU + 32 + 4 * w + g];
    const float hv = rb(gelu_tanh(rb(ga))) * rb(up);
    hp = pk(hv, hv);
    asm volatile("" : "+v"(hp));
  }
  lockstep();
  pace<7>(); SLOT(13); ISSUE(13); lockstep();
  pace<6>(); SLOT(14);
  const int mo = WS_ML + (h * 8 + c8) * 16;
  v4u ml = ld16<AUX_SC1>(rws, mo);
  PIN();
  ISSUE(14); lockstep();
  pace<7>(); SLOT(15); ISSUE(15);
  const int gd = 128 * rgl + 16 * w, hl = (gd * 683) >> 17;
  int o0 = WS_OP + ((4 * xg + hl) * 8 + u) * P_STRIDE + ((gd - 192 * hl) / 2 + c8) * 16;
  int ost = lane < 16 ? ob + (16 * w + lane) * 16 : OOB, yst = WS_YP + yb + lane * 16;
  asm volatile("" : "+v"(o0), "+v"(ost), "+v"(yst));
  lockstep();
  if (__builtin_expect(missing(ml, tag) != 0, 0)) {
    unsigned spins = 0;
#pragma unroll 1
    do { reload1(ml, rws, mo); } while (missing(ml, tag) != 0 && ++spins < SPIN_CAP);
  }
  {
    const float ms = asf(ml.x), M = max8(ms);
    const float inv = 1.f / sum8(__builtin_amdgcn_exp2f((ms - M) * LOG2E) * asf(ml.z));
#pragma unroll
    for (int i = 0; i < 8; ++i) sc[i] = __builtin_amdgcn_exp2f((sc[i] - M) * LOG2E) * inv;
  }
  f32x4 acc[3] = {f32x4{0.f, 0.f, 0.f, 0.f}, f32x4{0.f, 0.f, 0.f, 0.f}, f32x4{0.f, 0.f, 0.f, 0.f}};
#define PV(i) do { const v4u dp_ = diag(sc[i], mask); \
    _Pragma("unroll") for (int t_ = 0; t_ < 3; ++t_) acc[t_] = mfma(dp_, kv[24 + 3 * (i) + t_], acc[t_]); PIN(); } while (0)
#pragma unroll
  for (int i = 0; i < 4; ++i) PV(i);
  SLOT(16);
  const v4u wdr = ld16<AUX_NT>(rd, (4 * w + g) * 1280 + n16 * 16);
  PIN();
  v4u wor[4];
#pragma unroll
  for (int r = 0; r < 4; ++r) wor[r] = ld16<AUX_NT>(ro, (16 * w + 8 * (g >> 1) + (g & 1)) * 1280 + n16 * 16, r * 2560);
  PIN();
#pragma unroll
  for (int i = 4; i < 8; ++i) PV(i);
  if (valid) {
#pragma unroll
    for (int t = 0; t < 3; ++t) *(f32x4*)(redp + 64 * t) = acc[t];
  }
  wg_barrier();
  if (w < 6) publish(rws, ost, SUM192(), tag);
  const float* const sum2p = lds + L_RED2 + (lane >> 5) * 512 + 4 * (lane & 31);
#define SUM128(o) ({ const f32x4 s_ = (*(const f32x4*)(sum2p + (o)) + *(const f32x4*)(sum2p + (o) + 128)) + (*(const f32x4*)(sum2p + (o) + 256) + *(const f32x4*)(sum2p + (o) + 384)); \
    const auto a_ = __builtin_amdgcn_permlane32_swap(asu(s_[0]), asu(s_[2]), false, false), b_ = __builtin_amdgcn_permlane32_swap(asu(s_[1]), asu(s_[3]), false, false); \
    f32x2{asf(a_[0]), asf(b_[0])} + f32x2{asf(a_[1]), asf(b_[1])}; })
  unsigned spins = 0;
  if (tile) {
    const f32x4 z = mfma(v4u{hp, hp, hp, hp} & mask, wdr, f32x4{0.f, 0.f, 0.f, 0.f});
    if (lane < 32) *(f32x4*)(red2p + 1024) = z;
    wg_barrier();
    if (w == 7) publish(rws, yst + (WS_ZP - WS_YP), SUM128(1024), tag);
    if (w != 6) pace<1>(); else pace<0>();
    v4u e0;
#pragma unroll 1
    for (;;) {
      reload1(e0, rws, o0);
      if (missing(e0, tag) == 0) break;
      if (++spins >= SPIN_CAP) break;
    }
    float t0 = asf(e0.x), t1 = asf(e0.z);
    t0 += dpp<ROR8>(t0); t1 += dpp<ROR8>(t1);
    const float ov = sum_rows_xy(t0, t1);
    unsigned op = pk(ov, ov);
    op = (unsigned)__builtin_amdgcn_update_dpp((int)op, (int)op, ROR4, 0xc, 0xf, false);
    f32x4 y = mfma(diag_lane<0>(op, mask), wor[0], f32x4{0.f, 0.f, 0.f, 0.f});
    y = mfma(diag_lane<1>(op, mask), wor[1], y);
    y = mfma(diag_lane<2>(op, mask), wor[2], y);
    y = mfma(diag_lane<3>(op, mask), wor[3], y);
    if (lane < 32) *(f32x4*)red2p = y;
    wg_barrier();
    if (w == 0) publish(rws, yst, SUM128(0), tag);
  } else if (w >= 3) {
    const int unit = 5 * (xg + 8 * (idx - 30)) + w - 3;
    const int slot = 4 * (unit & 15) + (lane & 3);
    const int col = 128 * (unit >> 4) + 4 * (slot & 31) + 2 * (slot >> 5) + (g & 1);
    const int fo = ((unit >> 4) * 48 + (lane >> 2)) * Y_STRIDE + slot * 16;
    float xo = bflo(__builtin_amdgcn_raw_buffer_load_b16(rx, col * 2, 0, 0));
    v4u f0, f1, f2;
#pragma unroll 1
    do {
      reload3(f0, f1, f2, rws, WS_ZP + fo, WS_ZP + fo + 16 * Y_STRIDE, WS_ZP + fo + 32 * Y_STRIDE);
    } while ((missing(f0, tag) | missing(f1, tag) | missing(f2, tag)) != 0 && ++spins < SPIN_CAP);
    const float zz = colsum3(f0, f1, f2);
    float gt = rb(zz / (1.f + __builtin_amdgcn_exp2f(-zz * LOG2E)));
    int oc = (lane < 32 && n16 < 4) ? col * 2 : OOB;
    int y0 = WS_YP + fo, y1 = y0 + 16 * Y_STRIDE, y2 = y0 + 32 * Y_STRIDE;
    asm volatile("" : "+v"(gt), "+v"(xo), "+v"(oc), "+v"(y0), "+v"(y1), "+v"(y2));
    const rsrc_t rout = mk(out, D * 2);
#pragma unroll 1
    for (;;) {
      reload3(f0, f1, f2, rws, y0, y1, y2);
      if ((missing(f0, tag) | missing(f1, tag) | missing(f2, tag)) == 0) break;
      if (++spins >= SPIN_CAP) break;
    }
    __builtin_amdgcn_raw_buffer_store_b16((u16)pk(xo + rb(colsum3(f0, f1, f2) * gt), 0.f), rout, oc, 0, AUX_SC1);
    if (unit == 0) __builtin_amdgcn_raw_buffer_store_b32(tag, rws, lane == 0 ? WS_EPOCH : OOB, 0, AUX_SC1);
  }
}

extern "C" void kernel_launch(void* const* d_in, const int* in_sizes, int n_in, void* d_out, int out_size, void* d_ws, size_t ws_size,
                              hipStream_t stream) {
  fused_decode<<<256, 512, 0, stream>>>((const u16*)d_in[0], (const u16*)d_in[1], (const u16*)d_in[2], (const u16*)d_in[3],
                                        (const u16*)d_in[4], (const u16*)d_in[6], (const u16*)d_in[7], (const u16*)d_in[8],
                                        (u16*)d_out, (unsigned char*)d_ws);
}
```
